```python
import torch

def _softmax(x, axis=-1):
    return torch.softmax(x, dim=axis)
def _gelu(x):
    return torch.nn.functional.gelu(x, approximate="tanh")
def _silu(x):
    return torch.nn.functional.silu(x)
def _sigmoid(x):
    return torch.sigmoid(x)
def _layernorm(x, axis=-1, eps=1e-5):
    m = x.mean(dim=axis, keepdim=True)
    v = x.var(dim=axis, keepdim=True, unbiased=False)
    return (x - m) / torch.sqrt(v + eps)
def _scatter_add(tgt, idx, src, axis=0):
    return tgt.clone().index_add_(axis, idx, src)
def _segment_sum(src, seg, n):
    out = torch.zeros((n,) + src.shape[1:], dtype=src.dtype)
    return out.index_add_(0, seg, src)
def _pad(x, pw):
    flat = tuple(v for pair in reversed(pw) for v in pair)
    return torch.nn.functional.pad(x, flat)

kernel_name = 'gated_mix__ssm_conv_s4__softmax_full_mha__prompt'

def setup_inputs():

    _w = (lambda a: (lambda t: t.to(torch.bfloat16) if t.is_floating_point() else t)(torch.as_tensor(a)))
    def _A_setup():
        B, L, D, N = 1, 2048, 512, 128
        A_re = -(lambda _x: torch.exp(torch.as_tensor(_x)))((lambda *s: torch.randn(*s, dtype=torch.bfloat16) * 6.4)(D, N))
        A_im = torch.pi * (lambda *s: torch.randn(*s, dtype=torch.bfloat16) * 0.1)(D, N)
        B_re = (lambda *s: torch.randn(*s, dtype=torch.bfloat16) * 0.2)(D, N)
        B_im = (lambda *s: torch.randn(*s, dtype=torch.bfloat16) * 0.2)(D, N)
        C_re = (lambda *s: torch.randn(*s, dtype=torch.bfloat16) * 0.2)(D, N)
        C_im = (lambda *s: torch.randn(*s, dtype=torch.bfloat16) * 0.2)(D, N)
        log_dt = (lambda *s: torch.randn(*s, dtype=torch.bfloat16) * 0.1)(D) * 0.5 - 4.0
        Dskip = (lambda *s: torch.randn(*s, dtype=torch.bfloat16) * 0.1)(D)
        return {
            "u": _w((lambda *s: torch.randn(*s, dtype=torch.bfloat16) * 0.05)(B, L, D)),
            "A_re": _w(A_re),
            "A_im": _w(A_im),
            "B_re": _w(B_re),
            "B_im": _w(B_im),
            "C_re": _w(C_re),
            "C_im": _w(C_im),
            "log_dt": _w(log_dt),
            "Dskip": _w(Dskip),
        }
    _A_in = _A_setup()

    def _B_setup():
        B, S, D = 1, 2048, 512
        return {
            "x": _w((lambda *s: torch.randn(*s, dtype=torch.bfloat16) * 0.05)(B, S, D)),
            "wqkv": _w((lambda *s: torch.randn(*s, dtype=torch.bfloat16) * 0.8)(D, 3 * D)),
            "wo": _w((lambda *s: torch.randn(*s, dtype=torch.bfloat16) * 0.05)(D, D)),
        }
    _B_in = _B_setup()

    return {
        "A_u": _A_in["u"],
        "A_A_re": _A_in["A_re"],
        "A_A_im": _A_in["A_im"],
        "A_B_re": _A_in["B_re"],
        "A_B_im": _A_in["B_im"],
        "A_C_re": _A_in["C_re"],
        "A_C_im": _A_in["C_im"],
        "A_log_dt": _A_in["log_dt"],
        "A_Dskip": _A_in["Dskip"],
        "B_x": _B_in["x"],
        "B_wqkv": _B_in["wqkv"],
        "B_wo": _B_in["wo"],
        "gate_w": _w((lambda *s: torch.randn(*s, dtype=torch.bfloat16) * 0.05)(512) * 16.0),
    }

def reference(A_u, A_A_re, A_A_im, A_B_re, A_B_im, A_C_re, A_C_im, A_log_dt, A_Dskip, B_x, B_wqkv, B_wo, gate_w):
    def _A_ref(u, A_re, A_im, B_re, B_im, C_re, C_im, log_dt, Dskip):
        Bsz, L, D = u.shape
        dt = torch.exp(log_dt)[:, None]
        A = A_re + 1j * A_im
        Bc = B_re + 1j * B_im
        Cc = C_re + 1j * C_im
        logdA = dt * A
        dB = dt * Bc
        l = (lambda x, dt: x.to(dt))(torch.arange(L), A_re.dtype)
        P = torch.exp(logdA[:, :, None] * l[None, None, :])
        K = torch.einsum("dn,dnl,dn->dl", Cc, P, dB).real
        L2 = 2 * L
        Kp = _pad(K, ((0, 0), (0, L)))
        up = (lambda x, *d: x.permute(*d))(u, 0, 2, 1)
        up = _pad(up, ((0, 0), (0, 0), (0, L)))
        Kf = (lambda x, axes=(-2,-1): torch.fft.rfft2(x.float(), dim=axes))(Kp[:, None, :], axes=(-2, -1))
        Uf = (lambda x, axes=(-2,-1): torch.fft.rfft2(x.float(), dim=axes))(up[:, :, None, :], axes=(-2, -1))
        Yf = Uf * Kf[None, :, :, :]
        y = (lambda x, s, axes=(-2,-1): torch.fft.irfft2(x, s=s, dim=axes).to(torch.bfloat16))(Yf, (1, L2), axes=(-2, -1))[:, :, 0, :L]
        y = (lambda x, *d: x.permute(*d))(y, 0, 2, 1) + u * Dskip[None, None, :]
        return y

    def _B_ref(x, wqkv, wo):
        B, S, D = x.shape
        H = 32; Dh = D // H
        qkv = (lambda x, *d: x.permute(*d))((x @ wqkv).reshape(B, S, 3, H, Dh), 2, 0, 3, 1, 4)
        q, k, v = qkv[0], qkv[1], qkv[2]
        scale = Dh ** -0.5
        scores = torch.einsum("bhqd,bhkd->bhqk", q, k) * scale
        p = _softmax(scores, axis=-1)
        o = torch.einsum("bhqk,bhkd->bhqd", p, v)
        return (lambda x, *d: x.permute(*d))(o, 0, 2, 1, 3).reshape(B, S, D) @ wo

    x = A_u
    g = _sigmoid((x * gate_w).sum(-1, keepdims=True))
    a = _A_ref(x, A_A_re, A_A_im, A_B_re, A_B_im, A_C_re, A_C_im, A_log_dt, A_Dskip)
    b = _B_ref(x, B_wqkv, B_wo)
    return g * a + (1 - g) * b
```

```cpp
#include <hip/hip_runtime.h>
#include <hip/hip_bf16.h>
#include <stdint.h>
#pragma clang diagnostic ignored "-Wint-to-pointer-cast"

#define DI __device__ __forceinline__
#define LAS __attribute__((address_space(3)))
#define GAS __attribute__((address_space(1)))
typedef float f32x2 __attribute__((ext_vector_type(2)));
typedef float f32x4 __attribute__((ext_vector_type(4)));
typedef float f32x8 __attribute__((ext_vector_type(8)));
typedef float f32x16 __attribute__((ext_vector_type(16)));
typedef unsigned u32x2 __attribute__((ext_vector_type(2)));
typedef unsigned u32x4 __attribute__((ext_vector_type(4)));
typedef short s16x4 __attribute__((ext_vector_type(4)));
typedef __bf16 bf16x2 __attribute__((ext_vector_type(2)));
typedef __bf16 bf16x8 __attribute__((ext_vector_type(8)));
typedef __bf16 bf16x16 __attribute__((ext_vector_type(16)));
typedef _Float16 f16x8 __attribute__((ext_vector_type(8)));
typedef unsigned u32x8 __attribute__((ext_vector_type(8)));
typedef unsigned u32x16 __attribute__((ext_vector_type(16)));
typedef float f32x32 __attribute__((ext_vector_type(32)));
typedef unsigned long long u64;
typedef const unsigned short* cptr16;

constexpr unsigned MB = 1u << 20;
constexpr unsigned WS_EPOCH = 0;
constexpr unsigned FS = 128;
constexpr unsigned WS_F1 = 32768;
constexpr unsigned WS_F2 = 524288;
constexpr unsigned WS_F3 = 98304;
constexpr unsigned WS_F4 = 131072;
constexpr unsigned WS_QF = 1 * MB;
constexpr unsigned WS_KV = 3 * MB;
constexpr unsigned WS_UT0 = 8 * MB;
constexpr unsigned WS_WF = 14 * MB;
constexpr unsigned WS_SPAN = 16 * MB;
constexpr unsigned UT_STRIDE = 4224;

constexpr unsigned SLOT = 28672;
constexpr unsigned TROW = 528;
constexpr unsigned L_U = 65536, U_CH = 16656;
constexpr unsigned L_TA = 98848, TA_CH = 21 * TROW, L_TB = 121024, TB_CH = 32 * TROW;
constexpr unsigned L_KI = L_TB, KI_IMG = 7680;
constexpr unsigned L_T = 154816, T_CH = 4112;
constexpr unsigned L_MISC = 163328;
constexpr unsigned L_EX = 136192;
constexpr unsigned LDS_BYTES = 163840;

constexpr int AUX_SC1 = 16;
constexpr unsigned SPIN_CAP = 1u << 18;

DI float bf2f(unsigned v) { return __uint_as_float(v << 16); }
DI unsigned cvtpk_bf16(float lo, float hi) { f32x2 v = {lo, hi}; return __builtin_bit_cast(unsigned, __builtin_convertvector(v, bf16x2)); }
DI float rnd_bf16(float x) { return __uint_as_float(cvtpk_bf16(x, x) << 16); }
template <class C> DI unsigned cvtpk_f16(C v) { unsigned r; asm("v_cvt_pk_f16_f32 %0, %1, %2" : "=v"(r) : "v"(v.x), "v"(v.y)); return r; }
template <class C> DI unsigned cvtpk_f16_conj(C v) { unsigned r; asm("v_cvt_pk_f16_f32 %0, %1, -%2" : "=v"(r) : "v"(v.x), "v"(v.y)); return r; }
extern "C" __device__ float gm_fmul_legacy(float, float) __asm("llvm.amdgcn.fmul.legacy");
DI float fmul(float a, float b) { return gm_fmul_legacy(a, b); }
struct cf { float x, y; };
DI cf cmul(cf a, cf b) {
    cf r;
    asm("v_mul_f32 %0, %2, %4\n\tv_mul_f32 %1, %3, %4\n\tv_fma_f32 %0, -%3, %5, %0\n\tv_fmac_f32 %1, %2, %5" : "=&v"(r.x), "=&v"(r.y) : "v"(a.x), "v"(a.y), "v"(b.x), "v"(b.y));
    return r;
}
DI void cmul4(cf& r0, cf& r1, cf& r2, cf& r3, cf a0, cf a1, cf a2, cf a3, cf b) {
    f32x2 t0, t1, t2, t3;
    asm("v_pk_mul_f32 %0, %4, %8 op_sel:[0,0] op_sel_hi:[1,0]\n\tv_pk_mul_f32 %1, %5, %8 op_sel:[0,0] op_sel_hi:[1,0]\n\t"
        "v_pk_mul_f32 %2, %6, %8 op_sel:[0,0] op_sel_hi:[1,0]\n\tv_pk_mul_f32 %3, %7, %8 op_sel:[0,0] op_sel_hi:[1,0]\n\t"
        "v_pk_fma_f32 %0, %4, %8, %0 op_sel:[1,1,0] op_sel_hi:[0,1,1] neg_lo:[1,0,0]\n\tv_pk_fma_f32 %1, %5, %8, %1 op_sel:[1,1,0] op_sel_hi:[0,1,1] neg_lo:[1,0,0]\n\t"
        "v_pk_fma_f32 %2, %6, %8, %2 op_sel:[1,1,0] op_sel_hi:[0,1,1] neg_lo:[1,0,0]\n\tv_pk_fma_f32 %3, %7, %8, %3 op_sel:[1,1,0] op_sel_hi:[0,1,1] neg_lo:[1,0,0]"
        : "=&v"(t0), "=&v"(t1), "=&v"(t2), "=&v"(t3)
        : "v"(f32x2{a0.x, a0.y}), "v"(f32x2{a1.x, a1.y}), "v"(f32x2{a2.x, a2.y}), "v"(f32x2{a3.x, a3.y}), "v"(f32x2{b.x, b.y}));
    r0 = cf{t0.x, t0.y}; r1 = cf{t1.x, t1.y}; r2 = cf{t2.x, t2.y}; r3 = cf{t3.x, t3.y};
}
template <class T> DI T lds_ld(unsigned a) { return *(LAS const T*)a; }
template <class T> DI void lds_st(unsigned a, T v) { *(LAS T*)a = v; }
DI u32x2 lds_tr(unsigned a) { return __builtin_bit_cast(u32x2, __builtin_amdgcn_ds_read_tr16_b64_v4i16((LAS s16x4*)a)); }
DI bf16x8 as_bf8(u32x4 v) { return __builtin_bit_cast(bf16x8, v); }
DI bf16x2 as_bf2(unsigned v) { return __builtin_bit_cast(bf16x2, v); }
DI f16x8 as_h8(u32x4 v) { return __builtin_bit_cast(f16x8, v); }

DI void dma16(unsigned ldsaddr, unsigned voff, const void* sbase) {
    asm volatile("s_mov_b32 m0, %0\n\ts_nop 0\n\tglobal_load_lds_dwordx4 %1, %2" ::"s"(ldsaddr), "v"(voff), "s"(sbase) : "memory");
}
DI void dma16_sc1(unsigned ldsaddr, unsigned voff, const void* sbase) {
    asm volatile("s_mov_b32 m0, %0\n\ts_nop 0\n\tglobal_load_lds_dwordx4 %1, %2 sc1" ::"s"(ldsaddr), "v"(voff), "s"(sbase) : "memory");
}
#define VMWAIT(n) asm volatile("s_waitcnt vmcnt(" #n ")" ::: "memory")
#define BARRIER() asm volatile("s_waitcnt lgkmcnt(0)\n\ts_barrier" ::: "memory")

DI void st16(__amdgpu_buffer_rsrc_t r, u32x4 v, unsigned voff, unsigned soff) {
    __builtin_amdgcn_raw_buffer_store_b128(v, r, voff, soff, AUX_SC1);
    asm volatile("s_nop 1" ::"v"(v) : "memory");
}
DI void st8(__amdgpu_buffer_rsrc_t r, u32x2 v, unsigned voff, unsigned soff) { __builtin_amdgcn_raw_buffer_store_b64(v, r, voff, soff, AUX_SC1); }
DI u32x4 ld16(__amdgpu_buffer_rsrc_t r, unsigned voff, unsigned soff) { return __builtin_amdgcn_raw_buffer_load_b128(r, voff, soff, AUX_SC1); }

DI u64 read_tag(const char* ws) {
    u64 e;
    asm volatile("s_load_dwordx2 %0, %1, 0x0\n\ts_waitcnt lgkmcnt(0)" : "=&s"(e) : "s"(ws) : "memory");
    return e * 6364136223846793005ull + 1442695040888963407ull;
}
DI void publish(char* ws, unsigned off, u64 tag, int lane) {
    if (lane < 16) __hip_atomic_store((GAS u64*)(ws + off + lane * 8), tag, __ATOMIC_RELAXED, __HIP_MEMORY_SCOPE_AGENT);
}
DI u64 ld_flag(__amdgpu_buffer_rsrc_t r, unsigned off) { const u32x2 a = __builtin_amdgcn_raw_buffer_load_b64(r, off, 0, AUX_SC1); return ((u64)a.y << 32) | a.x; }
template <int SLEEP> DI void wait_flags(__amdgpu_buffer_rsrc_t r, unsigned off, u64 tag) {
#pragma unroll 1
    for (unsigned spins = 0; spins < SPIN_CAP; ++spins) {
        asm volatile("" ::: "memory");
        if (__builtin_amdgcn_ballot_w64(ld_flag(r, off) != tag) == 0) break;
        __builtin_amdgcn_s_sleep(SLEEP);
    }
    asm volatile("" ::: "memory");
}
template <int CTRL> DI unsigned dpp_max(unsigned v) { return max(v, (unsigned)__builtin_amdgcn_update_dpp(0, (int)v, CTRL, 0xf, 0xf, true)); }
template <int CTRL> DI float dpp_rot(float v) { return __int_as_float(__builtin_amdgcn_update_dpp(0, __float_as_int(v), CTRL, 0xf, 0xf, true)); }
DI unsigned wave_max(unsigned v) {
    v = dpp_max<0x128>(v); v = dpp_max<0x124>(v); v = dpp_max<0x122>(v); v = dpp_max<0x121>(v);
    return max(max(__builtin_amdgcn_readlane(v, 0), __builtin_amdgcn_readlane(v, 16)), max(__builtin_amdgcn_readlane(v, 32), __builtin_amdgcn_readlane(v, 48)));
}
#define SGB(mask, n) __builtin_amdgcn_sched_group_barrier(mask, n, 0)

#define CONV_LOOP \
    "ds_read_b128 v[64:67], %[pa] offset:0\n\t" \
    "ds_read_b128 v[92:95], %[pb] offset:4800\n\t" \
    "ds_read_b128 v[88:91], %[pb] offset:3264\n\t" \
    "ds_read_b128 v[84:87], %[pb] offset:1728\n\t" \
    "ds_read_b128 v[80:83], %[pb] offset:192\n\t" \
    "ds_read_b128 v[68:71], %[pa] offset:1024\n\t" \
    "ds_read_b128 v[72:75], %[pa] offset:2048\n\t" \
    "ds_read_b128 v[76:79], %[pa] offset:3072\n\t" \
    "1:\n\t" \
    "s_waitcnt lgkmcnt(6)\n\t" \
    "v_mfma_f32_16x16x32_bf16 %[y3], v[64:67], v[92:95], %[y3]\n\t" \
    "ds_read_b128 v[96:99], %[pa] offset:64\n\t" \
    "s_waitcnt lgkmcnt(6)\n\t" \
    "v_mfma_f32_16x16x32_bf16 %[y2], v[64:67], v[88:91], %[y2]\n\t" \
    "ds_read_b128 v[124:127], %[pb] offset:4704\n\t" \
    "s_waitcnt lgkmcnt(6)\n\t" \
    "v_mfma_f32_16x16x32_bf16 %[y1], v[64:67], v[84:87], %[y1]\n\t" \
    "ds_read_b128 v[120:123], %[pb] offset:3168\n\t" \
    "s_waitcnt lgkmcnt(6)\n\t" \
    "v_mfma_f32_16x16x32_bf16 %[y0], v[64:67], v[80:83], %[y0]\n\t" \
    "ds_read_b128 v[116:119], %[pb] offset:1632\n\t" \
    "s_waitcnt lgkmcnt(6)\n\t" \
    "v_mfma_f32_16x16x32_bf16 %[y3], v[68:71], v[88:91], %[y3]\n\t" \
    "ds_read_b128 v[112:115], %[pb] offset:96\n\t" \
    "v_mfma_f32_16x16x32_bf16 %[y2], v[68:71], v[84:87], %[y2]\n\t" \
    "ds_read_b128 v[100:103], %[pa] offset:1088\n\t" \
    "v_mfma_f32_16x16x32_bf16 %[y1], v[68:71], v[80:83], %[y1]\n\t" \
    "ds_read_b128 v[104:107], %[pa] offset:2112\n\t" \
    "s_waitcnt lgkmcnt(8)\n\t" \
    "v_mfma_f32_16x16x32_bf16 %[y3], v[72:75], v[84:87], %[y3]\n\t" \
    "ds_read_b128 v[108:111], %[pa] offset:3136\n\t" \
    "v_mfma_f32_16x16x32_bf16 %[y2], v[72:75], v[80:83], %[y2]\n\t" \
    "s_waitcnt lgkmcnt(8)\n\t" \
    "v_mfma_f32_16x16x32_bf16 %[y3], v[76:79], v[80:83], %[y3]\n\t" \
    "s_waitcnt lgkmcnt(6)\n\t" \
    "v_mfma_f32_16x16x32_bf16 %[y3], v[96:99], v[124:127], %[y3]\n\t" \
    "ds_read_b128 v[64:67], %[pa] offset:128\n\t" \
    "s_waitcnt lgkmcnt(6)\n\t" \
    "v_mfma_f32_16x16x32_bf16 %[y2], v[96:99], v[120:123], %[y2]\n\t" \
    "ds_read_b128 v[92:95], %[pb] offset:4608\n\t" \
    "s_waitcnt lgkmcnt(6)\n\t" \
    "v_mfma_f32_16x16x32_bf16 %[y1], v[96:99], v[116:119], %[y1]\n\t" \
    "ds_read_b128 v[88:91], %[pb] offset:3072\n\t" \
    "s_waitcnt lgkmcnt(6)\n\t" \
    "v_mfma_f32_16x16x32_bf16 %[y0], v[96:99], v[112:115], %[y0]\n\t" \
    "ds_read_b128 v[84:87], %[pb] offset:1536\n\t" \
    "s_waitcnt lgkmcnt(6)\n\t" \
    "v_mfma_f32_16x16x32_bf16 %[y3], v[100:103], v[120:123], %[y3]\n\t" \
    "ds_read_b128 v[80:83], %[pb] offset:0\n\t" \
    "v_mfma_f32_16x16x32_bf16 %[y2], v[100:103], v[116:119], %[y2]\n\t" \
    "ds_read_b128 v[68:71], %[pa] offset:1152\n\t" \
    "v_mfma_f32_16x16x32_bf16 %[y1], v[100:103], v[112:115], %[y1]\n\t" \
    "ds_read_b128 v[72:75], %[pa] offset:2176\n\t" \
    "s_waitcnt lgkmcnt(8)\n\t" \
    "v_mfma_f32_16x16x32_bf16 %[y3], v[104:107], v[116:119], %[y3]\n\t" \
    "ds_read_b128 v[76:79], %[pa] offset:3200\n\t" \
    "v_mfma_f32_16x16x32_bf16 %[y2], v[104:107], v[112:115], %[y2]\n\t" \
    "s_waitcnt lgkmcnt(8)\n\t" \
    "v_mfma_f32_16x16x32_bf16 %[y3], v[108:111], v[112:115], %[y3]\n\t" \
    "v_add_u32 %[pa], 0x80, %[pa]\n\t" \
    "v_subrev_u32 %[pb], 0xc0, %[pb]\n\t" \
    "s_sub_u32 %[cnt], %[cnt], 1\n\t" \
    "s_cmp_lg_u32 %[cnt], 0\n\t" \
    "s_cbranch_scc1 1b\n\t" \
    "s_waitcnt lgkmcnt(0)\n\t" \
    "s_nop 7\n\t"
DI void conv_wave(unsigned pa, unsigned pb, f32x4 (&acc)[4]) {
    f32x4 y0 = {}, y1 = {}, y2 = {}, y3 = {};
    unsigned cnt = 8;
    asm volatile(CONV_LOOP
                 : [y0] "+v"(y0), [y1] "+v"(y1), [y2] "+v"(y2), [y3] "+v"(y3), [pa] "+v"(pa), [pb] "+v"(pb), [cnt] "+s"(cnt)
                 :
                 : "memory", "scc", "v64", "v65", "v66", "v67", "v68", "v69", "v70", "v71", "v72", "v73", "v74", "v75", "v76", "v77", "v78", "v79", "v80", "v81", "v82", "v83", "v84", "v85", "v86", "v87", "v88", "v89", "v90", "v91", "v92", "v93", "v94", "v95", "v96", "v97", "v98", "v99", "v100", "v101", "v102", "v103", "v104", "v105", "v106", "v107", "v108", "v109", "v110", "v111", "v112", "v113", "v114", "v115", "v116", "v117", "v118", "v119", "v120", "v121", "v122", "v123", "v124", "v125", "v126", "v127");
    acc[0] = y0; acc[1] = y1; acc[2] = y2; acc[3] = y3;
}
#define PROJ_BODY \
    "ds_read_b128 v[160:163], %[ob] offset:0\n\t" \
    "ds_read_b128 v[164:167], %[ob] offset:256\n\t" \
    "ds_read_b128 v[168:171], %[ob] offset:2048\n\t" \
    "ds_read_b128 v[172:175], %[ob] offset:2304\n\t" \
    "ds_read_b128 v[176:179], %[ob] offset:4096\n\t" \
    "ds_read_b128 v[180:183], %[ob] offset:4352\n\t" \
    "ds_read_b128 v[184:187], %[ob] offset:6144\n\t" \
    "ds_read_b128 v[188:191], %[ob] offset:6400\n\t" \
    "ds_read_b128 v[192:195], %[ob] offset:8192\n\t" \
    "ds_read_b128 v[196:199], %[ob] offset:8448\n\t" \
    "ds_read_b128 v[200:203], %[ob] offset:10240\n\t" \
    "ds_read_b128 v[204:207], %[ob] offset:10496\n\t" \
    "ds_read_b128 v[208:211], %[ob] offset:12288\n\t" \
    "ds_read_b128 v[212:215], %[ob] offset:12544\n\t" \
    "ds_read_b128 v[216:219], %[ob] offset:14336\n\t" \
    "ds_read_b128 v[220:223], %[ob] offset:14592\n\t" \
    "s_waitcnt lgkmcnt(15)\n\t" \
    "v_mfma_f32_16x16x32_bf16 %[a0], v[64:67], v[160:163], 0\n\t" \
    "ds_read_b128 v[160:163], %[ob] offset:16384\n\t" \
    "s_waitcnt lgkmcnt(15)\n\t" \
    "v_mfma_f32_16x16x32_bf16 %[a1], v[64:67], v[164:167], 0\n\t" \
    "ds_read_b128 v[164:167], %[ob] offset:16640\n\t" \
    "s_waitcnt lgkmcnt(15)\n\t" \
    "v_mfma_f32_16x16x32_bf16 %[a0], v[68:71], v[168:171], %[a0]\n\t" \
    "ds_read_b128 v[168:171], %[ob] offset:18432\n\t" \
    "s_waitcnt lgkmcnt(15)\n\t" \
    "v_mfma_f32_16x16x32_bf16 %[a1], v[68:71], v[172:175], %[a1]\n\t" \
    "ds_read_b128 v[172:175], %[ob] offset:18688\n\t" \
    "s_waitcnt lgkmcnt(15)\n\t" \
    "v_mfma_f32_16x16x32_bf16 %[a0], v[72:75], v[176:179], %[a0]\n\t" \
    "ds_read_b128 v[176:179], %[ob] offset:20480\n\t" \
    "s_waitcnt lgkmcnt(15)\n\t" \
    "v_mfma_f32_16x16x32_bf16 %[a1], v[72:75], v[180:183], %[a1]\n\t" \
    "ds_read_b128 v[180:183], %[ob] offset:20736\n\t" \
    "s_waitcnt lgkmcnt(15)\n\t" \
    "v_mfma_f32_16x16x32_bf16 %[a0], v[76:79], v[184:187], %[a0]\n\t" \
    "ds_read_b128 v[184:187], %[ob] offset:22528\n\t" \
    "s_waitcnt lgkmcnt(15)\n\t" \
    "v_mfma_f32_16x16x32_bf16 %[a1], v[76:79], v[188:191], %[a1]\n\t" \
    "ds_read_b128 v[188:191], %[ob] offset:22784\n\t" \
    "s_waitcnt lgkmcnt(15)\n\t" \
    "v_mfma_f32_16x16x32_bf16 %[a0], v[80:83], v[192:195], %[a0]\n\t" \
    "ds_read_b128 v[192:195], %[ob] offset:24576\n\t" \
    "s_waitcnt lgkmcnt(15)\n\t" \
    "v_mfma_f32_16x16x32_bf16 %[a1], v[80:83], v[196:199], %[a1]\n\t" \
    "ds_read_b128 v[196:199], %[ob] offset:24832\n\t" \
    "s_waitcnt lgkmcnt(15)\n\t" \
    "v_mfma_f32_16x16x32_bf16 %[a0], v[84:87], v[200:203], %[a0]\n\t" \
    "ds_read_b128 v[200:203], %[ob] offset:26624\n\t" \
    "s_waitcnt lgkmcnt(15)\n\t" \
    "v_mfma_f32_16x16x32_bf16 %[a1], v[84:87], v[204:207], %[a1]\n\t" \
    "ds_read_b128 v[204:207], %[ob] offset:26880\n\t" \
    "s_waitcnt lgkmcnt(15)\n\t" \
    "v_mfma_f32_16x16x32_bf16 %[a0], v[88:91], v[208:211], %[a0]\n\t" \
    "ds_read_b128 v[208:211], %[ob] offset:28672\n\t" \
    "s_waitcnt lgkmcnt(15)\n\t" \
    "v_mfma_f32_16x16x32_bf16 %[a1], v[88:91], v[212:215], %[a1]\n\t" \
    "ds_read_b128 v[212:215], %[ob] offset:28928\n\t" \
    "s_waitcnt lgkmcnt(15)\n\t" \
    "v_mfma_f32_16x16x32_bf16 %[a0], v[92:95], v[216:219], %[a0]\n\t" \
    "ds_read_b128 v[216:219], %[ob] offset:30720\n\t" \
    "s_waitcnt lgkmcnt(15)\n\t" \
    "v_mfma_f32_16x16x32_bf16 %[a1], v[92:95], v[220:223], %[a1]\n\t" \
    "ds_read_b128 v[220:223], %[ob] offset:30976\n\t" \
    "ds_read_b32 %[g0], %[ga]\n\t" \
    "ds_read_b32 %[g1], %[ga] offset:64\n\t" \
    "s_waitcnt lgkmcnt(15)\n\t" \
    "v_mfma_f32_16x16x32_bf16 %[a0], v[96:99], v[160:163], %[a0]\n\t" \
    "s_waitcnt lgkmcnt(15)\n\t" \
    "v_mfma_f32_16x16x32_bf16 %[a1], v[96:99], v[164:167], %[a1]\n\t" \
    "s_waitcnt lgkmcnt(15)\n\t" \
    "v_mfma_f32_16x16x32_bf16 %[a0], v[100:103], v[168:171], %[a0]\n\t" \
    "s_waitcnt lgkmcnt(14)\n\t" \
    "v_mfma_f32_16x16x32_bf16 %[a1], v[100:103], v[172:175], %[a1]\n\t" \
    "s_waitcnt lgkmcnt(13)\n\t" \
    "v_mfma_f32_16x16x32_bf16 %[a0], v[104:107], v[176:179], %[a0]\n\t" \
    "s_waitcnt lgkmcnt(12)\n\t" \
    "v_mfma_f32_16x16x32_bf16 %[a1], v[104:107], v[180:183], %[a1]\n\t" \
    "s_waitcnt lgkmcnt(11)\n\t" \
    "v_mfma_f32_16x16x32_bf16 %[a0], v[108:111], v[184:187], %[a0]\n\t" \
    "s_waitcnt lgkmcnt(10)\n\t" \
    "v_mfma_f32_16x16x32_bf16 %[a1], v[108:111], v[188:191], %[a1]\n\t" \
    "s_waitcnt lgkmcnt(9)\n\t" \
    "v_mfma_f32_16x16x32_bf16 %[a0], v[112:115], v[192:195], %[a0]\n\t" \
    "s_waitcnt lgkmcnt(8)\n\t" \
    "v_mfma_f32_16x16x32_bf16 %[a1], v[112:115], v[196:199], %[a1]\n\t" \
    "s_waitcnt lgkmcnt(7)\n\t" \
    "v_mfma_f32_16x16x32_bf16 %[a0], v[116:119], v[200:203], %[a0]\n\t" \
    "s_waitcnt lgkmcnt(6)\n\t" \
    "v_mfma_f32_16x16x32_bf16 %[a1], v[116:119], v[204:207], %[a1]\n\t" \
    "s_waitcnt lgkmcnt(5)\n\t" \
    "v_mfma_f32_16x16x32_bf16 %[a0], v[120:123], v[208:211], %[a0]\n\t" \
    "s_waitcnt lgkmcnt(4)\n\t" \
    "v_mfma_f32_16x16x32_bf16 %[a1], v[120:123], v[212:215], %[a1]\n\t" \
    "s_waitcnt lgkmcnt(3)\n\t" \
    "v_mfma_f32_16x16x32_bf16 %[a0], v[124:127], v[216:219], %[a0]\n\t" \
    "s_waitcnt lgkmcnt(2)\n\t" \
    "v_mfma_f32_16x16x32_bf16 %[a1], v[124:127], v[220:223], %[a1]\n\t" \
    "s_waitcnt lgkmcnt(0)\n\t" \
    "s_nop 6\n\t"
DI void lds_arrive(unsigned addr) {
    asm volatile("s_waitcnt lgkmcnt(0)\n\ts_mov_b64 exec, 1\n\tds_add_u32 %0, %1\n\ts_mov_b64 exec, -1" ::"v"(addr), "v"(1u) : "memory");
}
DI void lds_wait(unsigned addr, unsigned target) {
    unsigned v, sv, n = 8 * SPIN_CAP;
    asm volatile("1:\n\tds_read_b32 %0, %3\n\ts_waitcnt lgkmcnt(0)\n\tv_readfirstlane_b32 %1, %0\n\ts_sub_u32 %2, %2, 1\n\ts_cbranch_scc1 2f\n\t"
                 "s_cmp_ge_u32 %1, %4\n\ts_cbranch_scc1 2f\n\ts_sleep 1\n\ts_branch 1b\n2:"
                 : "=&v"(v), "=&s"(sv), "+s"(n) : "v"(addr), "s"(target) : "memory", "scc");
}
DI void sync4(unsigned addr, unsigned target) { lds_arrive(addr); lds_wait(addr, target); }
DI unsigned ldu16_raw(const void* sbase, unsigned voff) { unsigned r; asm volatile("global_load_ushort %0, %1, %2" : "=v"(r) : "v"(voff), "s"(sbase) : "memory"); return r; }
DI u32x4 ld16_raw(const void* sbase, unsigned voff) { u32x4 r; asm volatile("global_load_dwordx4 %0, %1, %2" : "=v"(r) : "v"(voff), "s"(sbase) : "memory"); return r; }
DI u32x4 ld16_raw_sc1(const void* sbase, unsigned voff) { u32x4 r; asm volatile("global_load_dwordx4 %0, %1, %2 sc1" : "=v"(r) : "v"(voff), "s"(sbase) : "memory"); return r; }

#define ATT_BODY \
    "v_mov_b32 v44, 0xdddddddd\n\t" \
    "ds_read_b128 v[232:235], v56\n\t" \
    "s_mov_b64 exec, %[msk]\n\t" \
    "ds_read_b128 v[236:239], v57\n\t" \
    "ds_read_b128 v[240:243], v57 offset:512\n\t" \
    "s_mov_b64 exec, -1\n\t" \
    "s_getpc_b64 s[94:95]\n\t" \
    ".Lga_pc:\n\t" \
    "s_add_u32 s94, s94, .Lga_a0-.Lga_pc\n\t" \
    "s_addc_u32 s95, s95, 0\n\t" \
    "s_waitcnt lgkmcnt(0)\n\t" \
    "v_mfma_f32_32x32x16_bf16 v[160:175], v[232:235], v[224:227], 0\n\t" \
    "s_nop 12\n\t" \
    "v_exp_f32 v160, v160\n\t" \
    "v_exp_f32 v161, v161\n\t" \
    ".Lga_p0:\n\t" \
    "v_exp_f32 v162, v162\n\t" \
    "v_exp_f32 v163, v163\n\t" \
    "v_exp_f32 v164, v164\n\t" \
    "v_mfma_f32_32x32x16_bf16 v[176:191], v[232:235], v[228:231], 0\n\t" \
    "ds_read_b128 v[232:235], v56 offset:2048\n\t" \
    "s_mov_b64 exec, %[msk]\n\t" \
    "ds_read_b128 v[244:247], v57 offset:2048\n\t" \
    "ds_read_b128 v[248:251], v57 offset:2560\n\t" \
    "s_mov_b64 exec, -1\n\t" \
    "v_exp_f32 v165, v165\n\t" \
    "v_exp_f32 v166, v166\n\t" \
    "v_exp_f32 v167, v167\n\t" \
    "v_exp_f32 v168, v168\n\t" \
    "v_exp_f32 v169, v169\n\t" \
    "v_exp_f32 v170, v170\n\t" \
    "v_exp_f32 v171, v171\n\t" \
    "v_smfmac_f32_32x32x32_bf16 v[192:207], v[236:239], v[160:167], v44\n\t" \
    "v_exp_f32 v172, v172\n\t" \
    "v_exp_f32 v173, v173\n\t" \
    "v_exp_f32 v174, v174\n\t" \
    "v_exp_f32 v175, v175\n\t" \
    "v_exp_f32 v176, v176\n\t" \
    "v_exp_f32 v177, v177\n\t" \
    "v_smfmac_f32_32x32x32_bf16 v[192:207], v[240:243], v[168:175], v44\n\t" \
    "v_exp_f32 v178, v178\n\t" \
    "v_exp_f32 v179, v179\n\t" \
    "v_exp_f32 v180, v180\n\t" \
    "s_waitcnt lgkmcnt(0)\n\t" \
    "v_mfma_f32_32x32x16_bf16 v[160:175], v[232:235], v[224:227], 0\n\t" \
    "v_exp_f32 v181, v181\n\t" \
    "v_exp_f32 v182, v182\n\t" \
    "v_exp_f32 v183, v183\n\t" \
    "v_exp_f32 v184, v184\n\t" \
    "v_exp_f32 v185, v185\n\t" \
    "v_exp_f32 v186, v186\n\t" \
    "v_exp_f32 v187, v187\n\t" \
    "v_smfmac_f32_32x32x32_bf16 v[208:223], v[236:239], v[176:183], v44\n\t" \
    "v_exp_f32 v188, v188\n\t" \
    "v_exp_f32 v189, v189\n\t" \
    "v_exp_f32 v190, v190\n\t" \
    "v_exp_f32 v191, v191\n\t" \
    "v_exp_f32 v160, v160\n\t" \
    "v_exp_f32 v161, v161\n\t" \
    "v_smfmac_f32_32x32x32_bf16 v[208:223], v[240:243], v[184:191], v44\n\t" \
    "v_exp_f32 v162, v162\n\t" \
    "v_exp_f32 v163, v163\n\t" \
    "v_exp_f32 v164, v164\n\t" \
    "v_mfma_f32_32x32x16_bf16 v[176:191], v[232:235], v[228:231], 0\n\t" \
    "ds_read_b128 v[232:235], v56 offset:4096\n\t" \
    "ds_read_b32 v45, v58\n\t" \
    "s_mov_b64 exec, %[msk]\n\t" \
    "ds_read_b128 v[236:239], v57 offset:4096\n\t" \
    "ds_read_b128 v[240:243], v57 offset:4608\n\t" \
    "s_mov_b64 exec, -1\n\t" \
    "v_exp_f32 v165, v165\n\t" \
    "v_exp_f32 v166, v166\n\t" \
    "v_exp_f32 v167, v167\n\t" \
    "v_exp_f32 v168, v168\n\t" \
    "v_exp_f32 v169, v169\n\t" \
    "v_exp_f32 v170, v170\n\t" \
    "v_exp_f32 v171, v171\n\t" \
    "v_smfmac_f32_32x32x32_bf16 v[192:207], v[244:247], v[160:167], v44\n\t" \
    "v_exp_f32 v172, v172\n\t" \
    "v_exp_f32 v173, v173\n\t" \
    "v_exp_f32 v174, v174\n\t" \
    "v_exp_f32 v175, v175\n\t" \
    "v_exp_f32 v176, v176\n\t" \
    "v_exp_f32 v177, v177\n\t" \
    "v_smfmac_f32_32x32x32_bf16 v[192:207], v[248:251], v[168:175], v44\n\t" \
    "v_exp_f32 v178, v178\n\t" \
    "v_exp_f32 v179, v179\n\t" \
    "v_exp_f32 v180, v180\n\t" \
    "s_waitcnt lgkmcnt(0)\n\t" \
    "v_mfma_f32_32x32x16_bf16 v[160:175], v[232:235], v[224:227], 0\n\t" \
    "v_exp_f32 v181, v181\n\t" \
    "v_exp_f32 v182, v182\n\t" \
    "v_exp_f32 v183, v183\n\t" \
    "v_exp_f32 v184, v184\n\t" \
    "v_exp_f32 v185, v185\n\t" \
    "v_exp_f32 v186, v186\n\t" \
    "v_exp_f32 v187, v187\n\t" \
    "v_smfmac_f32_32x32x32_bf16 v[208:223], v[244:247], v[176:183], v44\n\t" \
    "v_exp_f32 v188, v188\n\t" \
    "v_exp_f32 v189, v189\n\t" \
    "v_exp_f32 v190, v190\n\t" \
    "v_exp_f32 v191, v191\n\t" \
    "v_exp_f32 v160, v160\n\t" \
    "v_exp_f32 v161, v161\n\t" \
    "v_smfmac_f32_32x32x32_bf16 v[208:223], v[248:251], v[184:191], v44\n\t" \
    "s_sub_u32 %[rem], %[rem], 1\n\t" \
    "s_cmp_lg_u32 %[kh], 0\n\t" \
    "s_cbranch_scc1 .Lga_l0\n\t" \
    "v_readfirstlane_b32 %[sp], v45\n\t" \
    "s_setprio 0\n\t" \
    "s_cmp_ge_u32 %[rem], %[sp]\n\t" \
    "s_cbranch_scc0 .Lga_d0\n\t" \
    "s_setprio 2\n\t" \
    "s_setpc_b64 s[94:95]\n\t" \
    ".Lga_l0:\n\t" \
    "v_mov_b32 v46, %[rem]\n\t" \
    "ds_write_b32 v58, v46\n\t" \
    ".Lga_d0:\n\t" \
    "s_setpc_b64 s[94:95]\n\t" \
    ".Lga_p1:\n\t" \
    "v_exp_f32 v162, v162\n\t" \
    "v_exp_f32 v163, v163\n\t" \
    "v_exp_f32 v164, v164\n\t" \
    "v_mfma_f32_32x32x16_bf16 v[176:191], v[232:235], v[228:231], 0\n\t" \
    "ds_read_b128 v[232:235], v56 offset:6144\n\t" \
    "s_mov_b64 exec, %[msk]\n\t" \
    "ds_read_b128 v[244:247], v57 offset:6144\n\t" \
    "ds_read_b128 v[248:251], v57 offset:6656\n\t" \
    "s_mov_b64 exec, -1\n\t" \
    "v_exp_f32 v165, v165\n\t" \
    "v_exp_f32 v166, v166\n\t" \
    "v_exp_f32 v167, v167\n\t" \
    "v_exp_f32 v168, v168\n\t" \
    "v_exp_f32 v169, v169\n\t" \
    "v_exp_f32 v170, v170\n\t" \
    "v_exp_f32 v171, v171\n\t" \
    "v_smfmac_f32_32x32x32_bf16 v[192:207], v[236:239], v[160:167], v44\n\t" \
    "v_exp_f32 v172, v172\n\t" \
    "v_exp_f32 v173, v173\n\t" \
    "v_exp_f32 v174, v174\n\t" \
    "v_exp_f32 v175, v175\n\t" \
    "v_exp_f32 v176, v176\n\t" \
    "v_exp_f32 v177, v177\n\t" \
    "v_smfmac_f32_32x32x32_bf16 v[192:207], v[240:243], v[168:175], v44\n\t" \
    "v_exp_f32 v178, v178\n\t" \
    "v_exp_f32 v179, v179\n\t" \
    "v_exp_f32 v180, v180\n\t" \
    "s_waitcnt lgkmcnt(0)\n\t" \
    "v_mfma_f32_32x32x16_bf16 v[160:175], v[232:235], v[224:227], 0\n\t" \
    "v_exp_f32 v181, v181\n\t" \
    "v_exp_f32 v182, v182\n\t" \
    "v_exp_f32 v183, v183\n\t" \
    "v_exp_f32 v184, v184\n\t" \
    "v_exp_f32 v185, v185\n\t" \
    "v_exp_f32 v186, v186\n\t" \
    "v_exp_f32 v187, v187\n\t" \
    "v_smfmac_f32_32x32x32_bf16 v[208:223], v[236:239], v[176:183], v44\n\t" \
    "v_exp_f32 v188, v188\n\t" \
    "v_exp_f32 v189, v189\n\t" \
    "v_exp_f32 v190, v190\n\t" \
    "v_exp_f32 v191, v191\n\t" \
    "v_exp_f32 v160, v160\n\t" \
    "v_exp_f32 v161, v161\n\t" \
    "v_smfmac_f32_32x32x32_bf16 v[208:223], v[240:243], v[184:191], v44\n\t" \
    "v_exp_f32 v162, v162\n\t" \
    "v_exp_f32 v163, v163\n\t" \
    "v_exp_f32 v164, v164\n\t" \
    "v_mfma_f32_32x32x16_bf16 v[176:191], v[232:235], v[228:231], 0\n\t" \
    "ds_read_b128 v[232:235], v56 offset:8192\n\t" \
    "ds_read_b32 v45, v58\n\t" \
    "s_mov_b64 exec, %[msk]\n\t" \
    "ds_read_b128 v[236:239], v57 offset:8192\n\t" \
    "ds_read_b128 v[240:243], v57 offset:8704\n\t" \
    "s_mov_b64 exec, -1\n\t" \
    "v_exp_f32 v165, v165\n\t" \
    "v_exp_f32 v166, v166\n\t" \
    "v_exp_f32 v167, v167\n\t" \
    "v_exp_f32 v168, v168\n\t" \
    "v_exp_f32 v169, v169\n\t" \
    "v_exp_f32 v170, v170\n\t" \
    "v_exp_f32 v171, v171\n\t" \
    "v_smfmac_f32_32x32x32_bf16 v[192:207], v[244:247], v[160:167], v44\n\t" \
    "v_exp_f32 v172, v172\n\t" \
    "v_exp_f32 v173, v173\n\t" \
    "v_exp_f32 v174, v174\n\t" \
    "v_exp_f32 v175, v175\n\t" \
    "v_exp_f32 v176, v176\n\t" \
    "v_exp_f32 v177, v177\n\t" \
    "v_smfmac_f32_32x32x32_bf16 v[192:207], v[248:251], v[168:175], v44\n\t" \
    "v_exp_f32 v178, v178\n\t" \
    "v_exp_f32 v179, v179\n\t" \
    "v_exp_f32 v180, v180\n\t" \
    "s_waitcnt lgkmcnt(0)\n\t" \
    "v_mfma_f32_32x32x16_bf16 v[160:175], v[232:235], v[224:227], 0\n\t" \
    "v_exp_f32 v181, v181\n\t" \
    "v_exp_f32 v182, v182\n\t" \
    "v_exp_f32 v183, v183\n\t" \
    "v_exp_f32 v184, v184\n\t" \
    "v_exp_f32 v185, v185\n\t" \
    "v_exp_f32 v186, v186\n\t" \
    "v_exp_f32 v187, v187\n\t" \
    "v_smfmac_f32_32x32x32_bf16 v[208:223], v[244:247], v[176:183], v44\n\t" \
    "v_exp_f32 v188, v188\n\t" \
    "v_exp_f32 v189, v189\n\t" \
    "v_exp_f32 v190, v190\n\t" \
    "v_exp_f32 v191, v191\n\t" \
    "v_exp_f32 v160, v160\n\t" \
    "v_exp_f32 v161, v161\n\t" \
    "v_smfmac_f32_32x32x32_bf16 v[208:223], v[248:251], v[184:191], v44\n\t" \
    "v_add_u32 v56, 0x2000, v56\n\t" \
    "v_add_u32 v57, 0x2000, v57\n\t" \
    "s_sub_u32 %[rem], %[rem], 1\n\t" \
    "s_cmp_lg_u32 %[kh], 0\n\t" \
    "s_cbranch_scc1 .Lga_l1\n\t" \
    "v_readfirstlane_b32 %[sp], v45\n\t" \
    "s_setprio 0\n\t" \
    "s_cmp_ge_u32 %[rem], %[sp]\n\t" \
    "s_cbranch_scc0 .Lga_d1\n\t" \
    "s_setprio 2\n\t" \
    "s_setpc_b64 s[94:95]\n\t" \
    ".Lga_l1:\n\t" \
    "v_mov_b32 v46, %[rem]\n\t" \
    "ds_write_b32 v58, v46\n\t" \
    ".Lga_d1:\n\t" \
    "s_setpc_b64 s[94:95]\n\t" \
    ".Lga_a0:\n\t" \
    "s_mov_b32 m0, %[m0b]\n\t" \
    "s_add_u32 %[m0b], %[m0b], 0x1000\n\t" \
    "global_load_lds_dwordx4 v48, %[ws] sc1\n\t" \
    "v_add_u32 v48, 0x1000, v48\n\t" \
    "s_mov_b32 m0, %[m0b]\n\t" \
    "s_add_u32 %[m0b], %[m0b], 0x1000\n\t" \
    "global_load_lds_dwordx4 v48, %[ws] sc1\n\t" \
    "v_add_u32 v48, 0x1000, v48\n\t" \
    "s_mov_b32 m0, %[m0b]\n\t" \
    "s_add_u32 %[m0b], %[m0b], 0x1000\n\t" \
    "global_load_lds_dwordx4 v48, %[ws] sc1\n\t" \
    "v_add_u32 v48, 0x1000, v48\n\t" \
    "s_add_u32 s94, s94, .Lga_a1-.Lga_a0\n\t" \
    "s_addc_u32 s95, s95, 0\n\t" \
    "s_branch .Lga_p1\n\t" \
    ".Lga_a1:\n\t" \
    "s_waitcnt vmcnt(3)\n\t" \
    "v_mov_b32 v61, 1\n\t" \
    "s_add_u32 %[rb], %[rb], 4\n\t" \
    "s_mov_b64 exec, 1\n\t" \
    "ds_add_u32 v59, v61\n\t" \
    "s_mov_b64 exec, -1\n\t" \
    "s_mov_b32 %[sn], 0x200000\n\t" \
    ".Lga_w1:\n\t" \
    "ds_read_b32 v60, v59\n\t" \
    "s_waitcnt lgkmcnt(0)\n\t" \
    "v_readfirstlane_b32 %[sp], v60\n\t" \
    "s_sub_u32 %[sn], %[sn], 1\n\t" \
    "s_cbranch_scc1 .Lga_x1\n\t" \
    "s_cmp_ge_u32 %[sp], %[rb]\n\t" \
    "s_cbranch_scc1 .Lga_x1\n\t" \
    "s_sleep 1\n\t" \
    "s_branch .Lga_w1\n\t" \
    ".Lga_x1:\n\t" \
    "s_add_u32 s94, s94, .Lga_a2-.Lga_a1\n\t" \
    "s_addc_u32 s95, s95, 0\n\t" \
    "s_branch .Lga_p0\n\t" \
    ".Lga_a2:\n\t" \
    "s_mov_b32 m0, %[m0b]\n\t" \
    "s_add_u32 %[m0b], %[m0b], 0x1000\n\t" \
    "global_load_lds_dwordx4 v48, %[ws] sc1\n\t" \
    "v_add_u32 v48, 0x1000, v48\n\t" \
    "s_mov_b32 m0, %[m0b]\n\t" \
    "s_add_u32 %[m0b], %[m0b], 0x1000\n\t" \
    "global_load_lds_dwordx4 v48, %[ws] sc1\n\t" \
    "v_add_u32 v48, 0x1000, v48\n\t" \
    "s_mov_b32 m0, %[m0b]\n\t" \
    "s_add_u32 %[m0b], %[m0b], 0x1000\n\t" \
    "global_load_lds_dwordx4 v48, %[ws] sc1\n\t" \
    "v_add_u32 v48, 0x1000, v48\n\t" \
    "s_add_u32 s94, s94, .Lga_a3-.Lga_a2\n\t" \
    "s_addc_u32 s95, s95, 0\n\t" \
    "s_branch .Lga_p1\n\t" \
    ".Lga_a3:\n\t" \
    "s_mov_b32 m0, %[m0b]\n\t" \
    "s_add_u32 %[m0b], %[m0b], 0x1000\n\t" \
    "global_load_lds_dwordx4 v48, %[ws] sc1\n\t" \
    "v_add_u32 v48, 0x1000, v48\n\t" \
    "s_mov_b32 m0, %[m0b]\n\t" \
    "s_add_u32 %[m0b], %[m0b], 0x1000\n\t" \
    "global_load_lds_dwordx4 v48, %[ws] sc1\n\t" \
    "v_add_u32 v48, 0x1000, v48\n\t" \
    "s_mov_b32 m0, %[m0b]\n\t" \
    "s_add_u32 %[m0b], %[m0b], 0x1000\n\t" \
    "global_load_lds_dwordx4 v48, %[ws] sc1\n\t" \
    "v_add_u32 v48, 0x1000, v48\n\t" \
    "s_add_u32 s94, s94, .Lga_a4-.Lga_a3\n\t" \
    "s_addc_u32 s95, s95, 0\n\t" \
    "s_branch .Lga_p0\n\t" \
    ".Lga_a4:\n\t" \
    "s_add_u32 s94, s94, .Lga_a5-.Lga_a4\n\t" \
    "s_addc_u32 s95, s95, 0\n\t" \
    "s_branch .Lga_p1\n\t" \
    ".Lga_a5:\n\t" \
    "s_waitcnt vmcnt(0)\n\t" \
    "v_mov_b32 v61, 1\n\t" \
    "s_add_u32 %[rb], %[rb], 4\n\t" \
    "s_mov_b64 exec, 1\n\t" \
    "ds_add_u32 v59, v61\n\t" \
    "s_mov_b64 exec, -1\n\t" \
    "s_mov_b32 %[sn], 0x200000\n\t" \
    ".Lga_w2:\n\t" \
    "ds_read_b32 v60, v59\n\t" \
    "s_waitcnt lgkmcnt(0)\n\t" \
    "v_readfirstlane_b32 %[sp], v60\n\t" \
    "s_sub_u32 %[sn], %[sn], 1\n\t" \
    "s_cbranch_scc1 .Lga_x2\n\t" \
    "s_cmp_ge_u32 %[sp], %[rb]\n\t" \
    "s_cbranch_scc1 .Lga_x2\n\t" \
    "s_sleep 1\n\t" \
    "s_branch .Lga_w2\n\t" \
    ".Lga_x2:\n\t" \
    "s_cmp_eq_u32 %[pub], 0\n\t" \
    "s_cbranch_scc1 .Lga_np\n\t" \
    "v_lshrrev_b32 v46, 1, v55\n\t" \
    "v_mov_b64 v[60:61], %[tag]\n\t" \
    "v_add_u32 v46, %[pub], v46\n\t" \
    "s_mov_b64 exec, 0xffff\n\t" \
    "global_store_dwordx2 v46, v[60:61], %[ws] sc1\n\t" \
    "s_mov_b64 exec, -1\n\t" \
    ".Lga_np:\n\t" \
    "s_add_u32 s94, s94, .Lga_a6-.Lga_a5\n\t" \
    "s_addc_u32 s95, s95, 0\n\t" \
    "s_branch .Lga_p0\n\t" \
    ".Lga_a6:\n\t" \
    "global_load_dwordx4 v[128:131], v52, %[xb] offset:0\n\t" \
    "global_load_dwordx4 v[132:135], v52, %[xb] offset:1024\n\t" \
    "global_load_dwordx4 v[136:139], v52, %[xb] offset:2048\n\t" \
    "global_load_dwordx4 v[140:143], v52, %[xb] offset:3072\n\t" \
    "s_add_u32 s94, s94, .Lga_a7-.Lga_a6\n\t" \
    "s_addc_u32 s95, s95, 0\n\t" \
    "s_branch .Lga_p1\n\t" \
    ".Lga_a7:\n\t" \
    "global_load_dwordx4 v[144:147], v55, %[gw] offset:0\n\t" \
    "global_load_dwordx2 v[148:149], v53, %[xb] offset:0\n\t" \
    "v_add_u32 v53, 0x4000, v53\n\t" \
    "global_load_dwordx2 v[62:63], v54, %[pd] offset:0\n\t" \
    "global_load_dwordx2 v[150:151], v53, %[xb] offset:0\n\t" \
    "v_mov_b32 v61, %[lc]\n\t" \
    "s_mov_b32 %[sn], 0x200000\n\t" \
    ".Lga_w3:\n\t" \
    "ds_read_b32 v60, v61\n\t" \
    "s_waitcnt lgkmcnt(0)\n\t" \
    "v_readfirstlane_b32 %[sp], v60\n\t" \
    "s_sub_u32 %[sn], %[sn], 1\n\t" \
    "s_cbranch_scc1 .Lga_x3\n\t" \
    "s_cmp_ge_u32 %[sp], 16\n\t" \
    "s_cbranch_scc1 .Lga_x3\n\t" \
    "s_sleep 1\n\t" \
    "s_branch .Lga_w3\n\t" \
    ".Lga_x3:\n\t" \
    "s_add_u32 s94, s94, .Lga_a8-.Lga_a7\n\t" \
    "s_addc_u32 s95, s95, 0\n\t" \
    "s_branch .Lga_p0\n\t" \
    ".Lga_a8:\n\t" \
    "global_load_dwordx2 v[60:61], v51, %[ws] offset:0 sc1\n\t" \
    "s_add_u32 s94, s94, .Lga_a9-.Lga_a8\n\t" \
    "s_addc_u32 s95, s95, 0\n\t" \
    "s_branch .Lga_p1\n\t" \
    ".Lga_a9:\n\t" \
    "s_mov_b32 %[sn], 0x40000\n\t" \
    ".Lga_f:\n\t" \
    "s_waitcnt vmcnt(0)\n\t" \
    "v_cmp_ne_u64 vcc, %[tag], v[60:61]\n\t" \
    "s_sub_u32 %[sn], %[sn], 1\n\t" \
    "s_cbranch_scc1 .Lga_g\n\t" \
    "s_cbranch_vccz .Lga_g\n\t" \
    "s_sleep 4\n\t" \
    "global_load_dwordx2 v[60:61], v51, %[ws] offset:0 sc1\n\t" \
    "s_branch .Lga_f\n\t" \
    ".Lga_g:\n\t" \
    "global_load_dwordx4 v[64:67], v49, %[ws] offset:0 sc1\n\t" \
    "global_load_dwordx4 v[68:71], v49, %[ws] offset:1024 sc1\n\t" \
    "global_load_dwordx4 v[72:75], v49, %[ws] offset:2048 sc1\n\t" \
    "global_load_dwordx4 v[76:79], v49, %[ws] offset:3072 sc1\n\t" \
    "v_add_u32 v49, 0x1000, v49\n\t" \
    "s_add_u32 s94, s94, .Lga_a10-.Lga_a9\n\t" \
    "s_addc_u32 s95, s95, 0\n\t" \
    "s_branch .Lga_p0\n\t" \
    ".Lga_a10:\n\t" \
    "global_load_dwordx4 v[80:83], v49, %[ws] offset:0 sc1\n\t" \
    "global_load_dwordx4 v[84:87], v49, %[ws] offset:1024 sc1\n\t" \
    "global_load_dwordx4 v[88:91], v49, %[ws] offset:2048 sc1\n\t" \
    "global_load_dwordx4 v[92:95], v49, %[ws] offset:3072 sc1\n\t" \
    "v_add_u32 v49, 0x1000, v49\n\t" \
    "s_add_u32 s94, s94, .Lga_a11-.Lga_a10\n\t" \
    "s_addc_u32 s95, s95, 0\n\t" \
    "s_branch .Lga_p1\n\t" \
    ".Lga_a11:\n\t" \
    "global_load_dwordx4 v[96:99], v49, %[ws] offset:0 sc1\n\t" \
    "global_load_dwordx4 v[100:103], v49, %[ws] offset:1024 sc1\n\t" \
    "global_load_dwordx4 v[104:107], v49, %[ws] offset:2048 sc1\n\t" \
    "global_load_dwordx4 v[108:111], v49, %[ws] offset:3072 sc1\n\t" \
    "v_add_u32 v49, 0x1000, v49\n\t" \
    "s_add_u32 s94, s94, .Lga_a12-.Lga_a11\n\t" \
    "s_addc_u32 s95, s95, 0\n\t" \
    "s_branch .Lga_p0\n\t" \
    ".Lga_a12:\n\t" \
    "global_load_dwordx4 v[112:115], v49, %[ws] offset:0 sc1\n\t" \
    "global_load_dwordx4 v[116:119], v49, %[ws] offset:1024 sc1\n\t" \
    "global_load_dwordx4 v[120:123], v49, %[ws] offset:2048 sc1\n\t" \
    "global_load_dwordx4 v[124:127], v49, %[ws] offset:3072 sc1\n\t" \
    "v_add_u32 v49, 0x1000, v49\n\t" \
    "s_add_u32 s94, s94, .Lga_a13-.Lga_a12\n\t" \
    "s_addc_u32 s95, s95, 0\n\t" \
    "s_branch .Lga_p1\n\t" \
    ".Lga_a13:\n\t" \
    "global_load_ushort v152, v50, %[ws] sc1\n\t" \
    "global_load_ushort v156, v50, %[ws] offset:16 sc1\n\t" \
    "v_add_u32 v50, 0x1080, v50\n\t" \
    "global_load_ushort v153, v50, %[ws] sc1\n\t" \
    "global_load_ushort v157, v50, %[ws] offset:16 sc1\n\t" \
    "v_add_u32 v50, 0x1080, v50\n\t" \
    "s_add_u32 s94, s94, .Lga_a14-.Lga_a13\n\t" \
    "s_addc_u32 s95, s95, 0\n\t" \
    "s_branch .Lga_p0\n\t" \
    ".Lga_a14:\n\t" \
    "global_load_ushort v154, v50, %[ws] sc1\n\t" \
    "global_load_ushort v158, v50, %[ws] offset:16 sc1\n\t" \
    "v_add_u32 v50, 0x1080, v50\n\t" \
    "global_load_ushort v155, v50, %[ws] sc1\n\t" \
    "global_load_ushort v159, v50, %[ws] offset:16 sc1\n\t" \
    "v_add_u32 v50, 0x1080, v50\n\t" \
    "s_add_u32 s94, s94, .Lga_a15-.Lga_a14\n\t" \
    "s_addc_u32 s95, s95, 0\n\t" \
    "s_branch .Lga_p1\n\t" \
    ".Lga_a15:\n\t" \
    "s_setprio 0\n\t" \
    "s_waitcnt vmcnt(0) lgkmcnt(0)\n\t" \
    "s_nop 10\n\t"

__global__ void __launch_bounds__(512, 2)
gated_mix_kernel(cptr16 a0, cptr16 a1, char* a2, cptr16 a3, cptr16 a4, cptr16 a5, cptr16 a6, cptr16 a7, cptr16 a8, cptr16 a9, cptr16 a10, cptr16 a11, cptr16 a12,
                 unsigned short* a13) {
    __shared__ __attribute__((aligned(1024))) char smem[LDS_BYTES];
    u32x16 ka; u32x8 kb; u32x4 kc;
    asm volatile(".Lgm_begin:\n\ts_load_dwordx16 %0, %3, 0x0\n\ts_load_dwordx8 %1, %3, 0x40\n\ts_load_dwordx4 %2, %3, 0x60"
                 : "=&s"(ka), "=&s"(kb), "=&s"(kc) : "s"(__builtin_amdgcn_kernarg_segment_ptr()), "v"((unsigned)(uintptr_t)(LAS char*)smem) : "memory");
    const int tid = threadIdx.x, lane = tid & 63, wv = __builtin_amdgcn_readfirstlane(tid >> 6);
    const int b = blockIdx.x, xcd = b & 7, bi = b >> 3;
    const int mt = (xcd & 3) * 4 + (bi & 3), nt = (xcd >> 2) * 8 + (bi >> 2);
    const int head = xcd + 8 * (bi & 3), qb = bi >> 2;
    const int l15 = lane & 15, g4 = lane >> 4, l31 = lane & 31, hh = lane >> 5;
    u32x4 cpf;
    if (wv == 2 || wv == 3) {
        u64 pc; unsigned len, back;
        asm volatile("s_getpc_b64 %0\n.Lgm_here:\n\ts_mov_b32 %1, .Lgm_end-.Lgm_begin\n\ts_mov_b32 %2, .Lgm_here-.Lgm_begin" : "=s"(pc), "=s"(len), "=s"(back));
        const unsigned co = bi * 1024 + (wv & 1) * 512 + l31 * 16;
        cpf = ld16_raw((const char*)((pc - back) & ~15ull), co + 16 <= len ? co : 0);
    }
    asm volatile("s_waitcnt lgkmcnt(0)" : "+s"(ka), "+s"(kb), "+s"(kc)::"memory");
#define KARG(v, i) ((u64)v[i] | (u64)v[(i) + 1] << 32)
    const cptr16 x = (cptr16)(GAS const unsigned short*)KARG(ka, 0), wqkv = (cptr16)(GAS const unsigned short*)KARG(ka, 2);
    char* const ws = (char*)(GAS char*)KARG(ka, 4);
    const cptr16 pAre = (cptr16)KARG(ka, 6), pAim = (cptr16)KARG(ka, 8), pLdt = (cptr16)KARG(ka, 10), pBre = (cptr16)KARG(ka, 12), pBim = (cptr16)KARG(ka, 14);
    const cptr16 pCre = (cptr16)KARG(kb, 0), pCim = (cptr16)KARG(kb, 2), wo = (cptr16)KARG(kb, 4), pD = (cptr16)KARG(kb, 6), gw = (cptr16)KARG(kc, 0);
    unsigned short* const out = (unsigned short*)(GAS unsigned short*)KARG(kc, 2);
    const __amdgpu_buffer_rsrc_t wsr = __builtin_amdgcn_make_buffer_rsrc(ws, 0, WS_SPAN, 0x00020000);

    u64 tag;

    const int c0 = (nt + 2 * mt) & 7, slice = 2 * c0 + (nt >> 3);
    const int T5 = 8 * qb + xcd, nq = bi & 3, n0 = 128 * nq + 16 * wv;
    const int kh = wv >> 2, qp = wv & 3;
    u32x4 q0, q1;
    u32x2 yq[4];
    const unsigned l16 = lane * 16;
    f32x32 OO;
    u32x16 VV;
    u32x4 P0, P1, P2;
    auto att_init = [&]() {
        const unsigned pad = (lane & 11) == 0 ? 0x3f803f80u : 0u;
        OO = f32x32{};
        VV = u32x16{pad, pad, pad, pad, pad, pad, pad, pad, pad, pad, pad, pad, pad, pad, pad, pad};
        P0 = u32x4{WS_KV + head * 131072 + kh * 65536 + qp * 1024 + l16, WS_WF + n0 * 1024 + l16,
                   WS_UT0 + 128 + (n0 + 4 * g4) * UT_STRIDE + (l15 & 1) * 2048 + 32 * T5 + (l15 & 14), WS_F3 + (64 * nq + lane) * FS};
        P1 = u32x4{4096 * wv + l16, (unsigned)(l15 * 1024 + (n0 + 4 * g4) * 2), (unsigned)((n0 + 4 * g4) * 2), l16};
        P2 = u32x4{65536 * kh + l16, (unsigned)(65536 * kh + 1024 + 256 * hh + 16 * l15), L_MISC + 192 + 4 * qp, kh ? L_MISC + 24 : L_MISC + 28};
        asm volatile("" : "+{v[192:223]}"(OO), "+{v[236:251]}"(VV), "+{v[48:51]}"(P0), "+{v[52:55]}"(P1), "+{v[56:59]}"(P2));
    };

    unsigned rAre, rAim, rBre, rBim, rCre, rCim, rLdt, wof[8];
    const int wf = 2 * b + (wv & 1);
    unsigned xo[4];
    if (wv >= 4) {
#pragma unroll
        for (int j = 0; j < 4; ++j) {
            xo[j] = (32 * (wv - 4) + 8 * j + (lane >> 3)) * 1024 + (((lane & 7) ^ (4 * (j & 1) + (lane >> 4))) << 4);
            dma16(3 * SLOT + (4 * (wv - 4) + j) * 1024, xo[j], (const char*)x + mt * 131072 + c0 * 128);
        }
        const unsigned pc_ = 2 * b + ((wv >> 1) & 1), po = (pc_ * 128 + 64 * (wv & 1) + lane) * 2;
        rAre = ldu16_raw(pAre, po); rAim = ldu16_raw(pAim, po); rLdt = ldu16_raw(pLdt, pc_ * 2);
        rBre = ldu16_raw(pBre, po); rBim = ldu16_raw(pBim, po); rCre = ldu16_raw(pCre, po); rCim = ldu16_raw(pCim, po);
    } else if (wv < 2) {
        const unsigned wb = ((32 * (wf & 15) + 16 * (g4 >> 1) + 4 * (g4 & 1)) * 512 + 16 * (wf >> 4) + l15) * 2;
#pragma unroll
        for (int j = 0; j < 8; ++j) wof[j] = ldu16_raw(wo, wb + ((j & 3) + 8 * (j >> 2)) * 1024);
    }
    int role = wv;
    asm volatile("" : "+s"(role));
    if (role < 4) {
        f32x16 acc[3] = {};
        u32x2 f1;
        {
            const int row = 32 * wv + l31;
            unsigned xa[4];
#pragma unroll
            for (int ks = 0; ks < 4; ++ks) xa[ks] = row * 128 + (((2 * ks + hh) ^ ((row >> 1) & 7)) << 4);
            unsigned wb[3], xh[4], wh[3];
            wb[0] = 16384 + (8 * hh + ((lane >> 2) & 3)) * 128 + (lane & 8) * 8 + (g4 & 1) * 32 + (lane & 3) * 8;
            wb[1] = wb[0] ^ 64;
            wb[2] = 24576 + (8 * hh + ((lane >> 2) & 3)) * 64 + (g4 & 1) * 32 + (lane & 3) * 8;
#pragma unroll
            for (int ks = 0; ks < 4; ++ks) { xh[ks] = xa[ks] + 2 * SLOT; asm("" : "+v"(xh[ks])); }
#pragma unroll
            for (int ns = 0; ns < 3; ++ns) { wh[ns] = wb[ns] + 2 * SLOT; asm("" : "+v"(wh[ns])); }
            const int tile = 4 * mt + wv;
            const bool isq = nt < 8;
            float sc = isq ? 0.25f * 1.44269504088896341f : 1.f;
            const unsigned sw = (l31 & 0x13) | ((l31 & 4) << 1) | ((l31 & 8) >> 1);
            unsigned voff = 512 * hh + 16 * (isq ? l31 : sw), vvoff = (l31 >> 4) * 131072 + 256 * hh + 16 * l15;
            unsigned qkb = isq ? WS_QF + (4 * nt * 64 + tile) * 1024 : WS_KV + (4 * (nt - 8) * 64 + tile) * 2048, qks = isq ? 65536 : 131072;
            unsigned vb_ = WS_KV + (2 * nt * 64 + tile) * 2048 + 1024;
            asm volatile("" : "+v"(voff), "+v"(vvoff), "+v"(sc), "+s"(qkb), "+s"(qks), "+s"(vb_));
            tag = read_tag(ws);
            BARRIER();
            u32x4 X[2][4];
            u32x2 Wt[2][4][3][2];
#pragma unroll
            for (int it = 0; it < 8; ++it) {
                {
                    const bool hi = (it & 3) < 2;
                    const unsigned so = SLOT * (1 - (it & 1));
#pragma unroll
                    for (int ks = 0; ks < 4; ++ks) {
                        X[it & 1][ks] = *(LAS const u32x4*)((LAS const char*)(hi ? xh[ks] : xa[ks]) + so);
#pragma unroll
                        for (int ns = 0; ns < 3; ++ns)
#pragma unroll
                            for (int u = 0; u < 2; ++u)
                                Wt[it & 1][ks][ns][u] = __builtin_bit_cast(u32x2, __builtin_amdgcn_ds_read_tr16_b64_v4i16((LAS s16x4*)((LAS const char*)(hi ? wh[ns] : wb[ns]) + so + (16 * ks + 4 * u) * (ns < 2 ? 128 : 64))));
                    }
                }
                if (it == 0) {
                    const int chh = wv & 1, q = (lane >> 2) & 3, p = lane & 3;
                    u32x4 tv[2];
#pragma unroll
                    for (int i = 0; i < 2; ++i) {
                        const int t8 = 64 * (wv >> 1) + 32 * i + 8 * g4;
                        u32x2 r[2];
#pragma unroll
                        for (int u = 0; u < 2; ++u) {
                            const int row = t8 + 4 * u + q;
                            r[u] = lds_tr(SLOT * 3 + row * 128 + (((4 * (nt >> 3) + 2 * chh + (p >> 1)) ^ ((row >> 1) & 7)) << 4) + 8 * (p & 1));
                        }
                        tv[i] = u32x4{r[0].x, r[0].y, r[1].x, r[1].y};
                    }
                    const bool up = lane & 8;
#pragma unroll
                    for (int j = 0; j < 4; ++j) {
                        const unsigned give = up ? tv[0][j] : tv[1][j];
                        const unsigned got = (unsigned)__builtin_amdgcn_update_dpp(0, (int)give, 0x128, 0xf, 0xf, true);
                        if (up) tv[0][j] = got; else tv[1][j] = got;
                    }
#pragma unroll
                    for (int i = 0; i < 2; ++i)
                        st16(wsr, tv[i], (32 * slice + 16 * chh + 8 * i + (lane & 7)) * UT_STRIDE + 128 + 2 * (128 * mt + 64 * (wv >> 1) + 4 * (lane & 8) + 8 * g4), WS_UT0);
                    if (mt == 0) {
                        u32x4 z = {0u, 0u, 0u, 0u};
                        asm volatile("" : "+v"(z));
                        st16(wsr, z, (32 * slice + 8 * wv + (lane >> 3)) * UT_STRIDE + 16 * (lane & 7), WS_UT0);
                    }
                }
                if (it == 6) asm volatile("global_load_dwordx2 %0, %1, %2 sc1" : "=v"(f1) : "v"(WS_F1 + ((b >> 4) * 16 + l15) * FS), "s"(ws) : "memory");
#pragma unroll
                for (int i = (it == 0 ? 6 : 0); i < 12; ++i) {
                    const int p = i < 6 ? (it - 1) & 1 : it & 1, ks = i < 6 ? 2 + i / 3 : (i - 6) / 3, ns = i % 3;
                    const u32x2 w0 = Wt[p][ks][ns][0], w1 = Wt[p][ks][ns][1];
                    const bf16x8 wfr = as_bf8(u32x4{w0.x, w0.y, w1.x, w1.y}), xf = as_bf8(X[p][ks]);
                    if (ns < 2) acc[ns] = __builtin_amdgcn_mfma_f32_32x32x16_bf16(wfr, xf, acc[ns], 0, 0, 0);
                    else        acc[ns] = __builtin_amdgcn_mfma_f32_32x32x16_bf16(xf, wfr, acc[ns], 0, 0, 0);
                }
                if (it > 0) {
#pragma unroll
                    for (int i = 0; i < 7; ++i) { SGB(0x8, 1); SGB(0x100, 4); }
                    SGB(0x8, 5);
                }
                __builtin_amdgcn_sched_barrier(0);
                if (it == 3)
                    asm volatile("s_waitcnt vmcnt(0)" : "+v"(cpf), "+v"(wof[0]), "+v"(wof[1]), "+v"(wof[2]), "+v"(wof[3]), "+v"(wof[4]), "+v"(wof[5]), "+v"(wof[6]), "+v"(wof[7])::"memory");
                if (it < 7) BARRIER();
                if (it == 3 && wv == 0) publish(ws, WS_F1 + (slice * 16 + mt) * FS, tag, lane);
            }
            auto last = [&](int ns) {
#pragma unroll
                for (int ks = 2; ks < 4; ++ks) {
                    const u32x2 w0 = Wt[1][ks][ns][0], w1 = Wt[1][ks][ns][1];
                    const bf16x8 wfr = as_bf8(u32x4{w0.x, w0.y, w1.x, w1.y}), xf = as_bf8(X[1][ks]);
                    if (ns < 2) acc[ns] = __builtin_amdgcn_mfma_f32_32x32x16_bf16(wfr, xf, acc[ns], 0, 0, 0);
                    else        acc[ns] = __builtin_amdgcn_mfma_f32_32x32x16_bf16(xf, wfr, acc[ns], 0, 0, 0);
                }
            };
            auto put = [&](int ns) {
#pragma unroll
                for (int e = 0; e < 2; ++e) {
                    u32x4 v;
#pragma unroll
                    for (int j = 0; j < 4; ++j)
                        v[j] = ns < 2 ? cvtpk_bf16(fmul(acc[ns][8 * e + 2 * j], sc), fmul(acc[ns][8 * e + 2 * j + 1], sc)) : cvtpk_bf16(acc[2][8 * e + 2 * j], acc[2][8 * e + 2 * j + 1]);
                    if (ns < 2) st16(wsr, v, voff, qkb + (2 * ns + e) * qks);
                    else        st16(wsr, v, vvoff, vb_ + 512 * e);
                }
            };
            __builtin_amdgcn_sched_barrier(0);
            last(2);
            __builtin_amdgcn_sched_barrier(0);
            last(0);
            __builtin_amdgcn_sched_barrier(0);
            last(1);
            __builtin_amdgcn_sched_barrier(0);
            put(2);
            __builtin_amdgcn_sched_barrier(0);
            put(0);
            __builtin_amdgcn_sched_barrier(0);
            if (wv < 2) {
                u32x4 w4 = {wof[0] | (wof[1] << 16), wof[2] | (wof[3] << 16), wof[4] | (wof[5] << 16), wof[6] | (wof[7] << 16)};
                st16(wsr, w4, lane * 16, WS_WF + wf * 1024);
            }
            put(1);
        }
        asm volatile("s_waitcnt vmcnt(0)" : "+v"(f1)::"memory");
        {
            unsigned old = 0;
            if (lane == 0) old = __hip_atomic_fetch_add((LAS unsigned*)(L_MISC + 16), 1u, __ATOMIC_RELAXED, __HIP_MEMORY_SCOPE_WORKGROUP);
            if (__builtin_amdgcn_readfirstlane(old) == 3) {
                u32x4 t4 = {(unsigned)tag, (unsigned)(tag >> 32), (unsigned)tag, (unsigned)(tag >> 32)};
                asm volatile("" : "+v"(t4));
                st16(wsr, t4, l16, WS_F2 + (nt * 16 + mt) * 8 * FS);
            }
        }
        if (wv != 0) {
            if (__builtin_amdgcn_ballot_w64((((u64)f1.y << 32) | f1.x) != tag) != 0) wait_flags<4>(wsr, WS_F1 + ((b >> 4) * 16 + l15) * FS, tag);
            if (wv >= 2) {
                const char* src = ws + WS_UT0 + 64 + (2 * b + wv - 2) * UT_STRIDE;
                const unsigned dst = L_U + (wv - 2) * U_CH;
#pragma unroll
                for (int p = 0; p < 16; ++p) dma16_sc1(dst + (p >> 2) * 4160 + (p >= 4 ? 16 : 0) + 1024 * (p & 3), l16 + 4 * (p >> 2) + 1024 * (p & 3), src);
            } else if (lane < 32) {
                const int c = lane >> 4, sg = (lane >> 2) & 3;
                lds_st<u32x4>(L_U + c * U_CH + sg * 4160 + (sg ? 16 : 0) + 4096 + 16 * (lane & 3), ld16(wsr, 64 + (2 * b + c) * UT_STRIDE + 4 * sg + 4096 + 16 * (lane & 3), WS_UT0));
            }
            VMWAIT(0);
            lds_arrive(L_MISC + 32);
        }
        if (b == 0 && wv == 1) {
#pragma unroll 1
            for (unsigned spins = 0; spins < SPIN_CAP; ++spins) {
                asm volatile("" ::: "memory");
                bool ok = true;
#pragma unroll
                for (int j = 0; j < 4; ++j) ok &= ld_flag(wsr, WS_F1 + (64 * j + lane) * FS) == tag;
                if (__builtin_amdgcn_ballot_w64(!ok) == 0) break;
                __builtin_amdgcn_s_sleep(4);
            }
            publish(ws, WS_EPOCH, tag, lane);
        }
        att_init();
    } else {
        const int lw = wv - 4, tl = tid - 256, cl = lw >> 1, ch = 2 * b + cl, n = 64 * (lw & 1) + lane;
        unsigned wo_[3];
#pragma unroll
        for (int j = 0; j < 2; ++j) wo_[j] = (16 * lw + 8 * j + (lane >> 3)) * 3072 + 128 * nt + (((lane & 7) ^ ((lane >> 2) & 4)) << 4);
        wo_[2] = (16 * lw + (lane >> 2)) * 3072 + 2048 + 64 * nt + 16 * (lane & 3);
        float dt, er, rv;
        unsigned ex, gb;
        cf W, W1, W2, W3, E1, E2, E4, E8, E16, E32, E64, G, G2, G4, v[8], a[21];
        LAS char* tb = (LAS char*)(L_TB + cl * TB_CH + n * 4);
        LAS char* ta_ = (LAS char*)(L_TA + cl * TA_CH + n * 4);
        auto E = [&](float m) -> cf {
            const float mag = __builtin_amdgcn_exp2f(er * m), f = __builtin_amdgcn_fractf(rv * m);
            return cf{fmul(mag, __builtin_amdgcn_cosf(f)), fmul(mag, __builtin_amdgcn_sinf(f))};
        };
        auto piece = [&](int k) {
            if (k == 0) dt = rnd_bf16(expf(bf2f(rLdt)));
            if (k == 1) {
                er = fmul(fmul(dt, bf2f(rAre)), 1.44269504088896341f); rv = fmul(fmul(dt, bf2f(rAim)), 0.15915494309189535f);
                W = cmul(cf{bf2f(rCre), bf2f(rCim)}, cf{fmul(dt, bf2f(rBre)), fmul(dt, bf2f(rBim))});
                ex = max(__float_as_uint(W.x) & 0x7fffffffu, __float_as_uint(W.y) & 0x7fffffffu);
            }
            if (k == 2) {
                ex = min(max(wave_max(ex) >> 23, 1u), 253u);
                const float sc = __uint_as_float((254u - ex) << 23);
                W = cf{fmul(W.x, sc), fmul(W.y, sc)};
                gb = 0;
                if (__builtin_expect(__builtin_amdgcn_ballot_w64(er > 0.f) != 0, 0)) {
                    const float gm = __uint_as_float(wave_max(__float_as_uint(fmaxf(er, 0.f))));
                    const unsigned ga = (unsigned)fminf(ceilf(gm * 2048.f), 126.f);
                    gb = (unsigned)fminf(ceilf(gm * 248.f), 126.f);
                    const float sa = __uint_as_float((127u - ga) << 23);
                    W = cf{fmul(W.x, sa), fmul(W.y, sa)};
                    ex = min(ex + ga + gb, 254u);
                }
                lds_st<float>(L_MISC + lw * 4, __uint_as_float(ex << 23));
            }
            if (k == 3) E8 = E(8.f);
            if (k == 4) E64 = E(64.f);
            if (k == 5) E1 = E(1.f);
            if (k == 6) G = E(256.f);
            if (k == 7) { E16 = cmul(E8, E8); E2 = cmul(E1, E1); G2 = cmul(G, G); W1 = cmul(W, E1); }
            if (k == 8) { v[3] = cmul(E16, E8); E32 = cmul(E16, E16); E4 = cmul(E2, E2); G4 = cmul(G2, G2); }
            if (k == 9) { W2 = cmul(W, E2); W3 = cmul(W1, E2); v[0] = cf{1.f, 0.f}; v[1] = E8; v[2] = E16; v[4] = E32; }
            if (k == 10) {
                v[5] = cmul(v[1], E32); v[6] = cmul(v[2], E32); v[7] = cmul(v[3], E32);
                if (__builtin_expect(__builtin_amdgcn_readfirstlane(gb) != 0, 0)) {
                    const float sb = __uint_as_float((127u - gb) << 23);
#pragma unroll
                    for (int j = 0; j < 8; ++j) v[j] = cf{fmul(v[j].x, sb), fmul(v[j].y, sb)};
                }
            }
            if (k >= 11 && k <= 18) {
                const int r = (k - 11) >> 1, j0 = 4 * ((k - 11) & 1);
                if (r > 0) cmul4(v[j0], v[j0 + 1], v[j0 + 2], v[j0 + 3], v[j0], v[j0 + 1], v[j0 + 2], v[j0 + 3], E64);
#pragma unroll
                for (int j = j0; j < j0 + 4; ++j) *(LAS unsigned*)(tb + (8 * r + j) * TROW) = cvtpk_f16_conj(v[j]);
            }
            if (k == 19) { a[0] = W; a[1] = W1; a[2] = W2; a[3] = W3; }
            if (k == 20) cmul4(a[4], a[5], a[6], a[7], W, W1, W2, W3, E4);
            if (k == 21) cmul4(a[8], a[9], a[10], a[11], W, W2, a[4], a[6], G);
            if (k == 22) cmul4(a[12], a[13], a[14], a[15], W, a[4], a[8], a[10], G2);
            if (k == 23) cmul4(a[16], a[17], a[18], a[19], W, a[8], a[12], a[14], G4);
            if (k == 24) a[20] = cmul(a[16], G4);
            if (k >= 19) {
#pragma unroll
                for (int j = 4 * (k - 19); j < 4 * (k - 19) + 4 && j < 21; ++j) *(LAS unsigned*)(ta_ + j * TROW) = cvtpk_f16(a[j]);
            }
            if (k == 12) __builtin_amdgcn_sched_barrier(0);
        };
        auto issue = [&](int it) {
            const int cc = (c0 + it) & 7;
            const unsigned so = SLOT * (3 - (it & 3));
            const char* xb = (const char*)x + mt * 131072 + cc * 128;
            const char* wb = (const char*)wqkv + cc * (64 * 3072);
#pragma unroll
            for (int j = 0; j < 7; ++j) {
                if (j < 2) dma16(so + 16384 + (2 * lw + j) * 1024, wo_[j], wb);
                else if (j == 2) dma16(so + 24576 + lw * 1024, wo_[2], wb);
                else if (it > 0) dma16(so + (4 * lw + j - 3) * 1024, xo[j - 3], xb);
            }
        };
        issue(0); issue(1);
        tag = read_tag(ws);
        if (tl < 2) lds_st<u32x4>(L_MISC + 16 + 16 * tl, u32x4{0u, 0u, 0u, 0u});
        if (tl < 4) lds_st<unsigned>(L_MISC + 192 + 4 * tl, 16u);
        asm volatile("s_waitcnt vmcnt(7)" : "+v"(rAre), "+v"(rAim), "+v"(rBre), "+v"(rBim), "+v"(rCre), "+v"(rCim), "+v"(rLdt)::"memory");
        BARRIER();
        issue(2);
        issue(3);
        VMWAIT(14); BARRIER(); issue(4);
        VMWAIT(14); BARRIER(); issue(5);
        VMWAIT(14); BARRIER(); issue(6);
        VMWAIT(14); BARRIER(); issue(7);
        VMWAIT(14); BARRIER();
        unsigned ta[5], s0, s1, s2, s3, tpa, tpb, tr0, tr1, kz, kw, abase, bbase;
        {
            const int rt = lw & 1;
#pragma unroll
            for (int r = 0; r < 4; ++r) {
                const unsigned pk0 = r | (4 + r) << 16, pk1 = (256 + 2 * r) | (512 + 4 * (r & 1) + 256 * (r >> 1)) << 16;
                const unsigned pk2 = (1024 + 256 * r) | 1025 << 16, pk3 = 1025 | 1025 << 16;
                const unsigned pk = rt ? (g4 < 2 ? pk2 : pk3) : (g4 < 2 ? pk0 : pk1);
                ta[r] = L_T + cl * T_CH + 2 * (((pk >> (16 * (g4 & 1))) & 0xffff) + 8 * l15);
            }
            ta[4] = ta[0];
            if (rt && lane == 16) ta[0] = L_T + cl * T_CH + 2 * 2048;
            tpa = L_TA + cl * TA_CH + (16 * rt + l15) * TROW + g4 * 16;
            tpb = L_TB + cl * TB_CH + l15 * TROW + g4 * 16;
            asm volatile("" : "+v"(ta[0]), "+v"(ta[1]), "+v"(ta[2]), "+v"(ta[3]), "+v"(ta[4]), "+v"(tpa), "+v"(tpb));
        }
        VMWAIT(7); BARRIER();
        {
            const int m = tl, seg = m < 32 ? 0 : m < 64 ? 1 : m < 128 ? 2 : 3, sg = (l15 + 1) & 3;
            s0 = seg == 0 ? 0x01000302u : seg == 1 ? 0x01000504u : 0x05040504u;
            s1 = seg == 0 ? 0x01000302u : seg < 3 ? 0x01000100u : (m & 1) ? 0x05040504u : 0x01000504u;
            s2 = seg == 0 ? 0x01000302u : seg < 3 ? 0x01000504u : 0x01000100u;
            s3 = seg == 0 ? 0x01000302u : 0x01000100u;
            tr0 = L_T + 16 * m; tr1 = tr0 - (m <= 32 ? 2 : 0);
            kz = L_KI + (tl >> 6) * KI_IMG + 96 * ((tl >> 2) & 15) + 16 * (tl & 3);
            kw = L_KI + 1536 + 96 * (m >> 2) + 16 * (m & 3);
            abase = L_U + cl * U_CH + sg * 4160 + (sg ? 16 : 0) + 16 * (3 - g4 + ((l15 + 1) >> 2));
            bbase = L_KI + (2 * cl + (lw & 1)) * KI_IMG + 1536 - 192 + 96 * l15 + 16 * g4;
            asm volatile("" : "+v"(s0), "+v"(s1), "+v"(s2), "+v"(s3), "+v"(tr0), "+v"(tr1), "+v"(kz), "+v"(kw), "+v"(abase), "+v"(bbase));
        }
        VMWAIT(0); BARRIER();
        __builtin_amdgcn_sched_barrier(0);
#pragma unroll
        for (int k = 0; k < 25; ++k) piece(k);
        __builtin_amdgcn_sched_barrier(0);
        __builtin_amdgcn_s_setprio(2);
        sync4(L_MISC + 24, 4);
        {
            LAS const char* pa = (LAS const char*)tpa;
            LAS const char* pb = (LAS const char*)tpb;
            u32x4 fa[8], fb[2][8];
#pragma unroll
            for (int ks = 0; ks < 8; ++ks) { fa[ks] = *(LAS const u32x4*)(pa + ks * 64); fb[0][ks] = *(LAS const u32x4*)(pb + ks * 64); fb[1][ks] = *(LAS const u32x4*)(pb + 16 * TROW + ks * 64); }
            const f32x2 inv = lds_ld<f32x2>(L_MISC + cl * 8);
            __builtin_amdgcn_sched_barrier(0);
            f32x4 a[2][2] = {};
#pragma unroll
            for (int ks = 0; ks < 4; ++ks)
#pragma unroll
                for (int i = 0; i < 4; ++i)
                    a[i & 1][i >> 1] = __builtin_amdgcn_mfma_f32_16x16x32_f16(as_h8(fa[4 * (i >> 1) + ks]), as_h8(fb[i & 1][4 * (i >> 1) + ks]), a[i & 1][i >> 1], 0, 0, 0);
#pragma unroll
            for (int r = 0; r < 4; ++r) {
                const unsigned pk = cvtpk_bf16(a[0][0][r] * inv.x + a[0][1][r] * inv.y, a[1][0][r] * inv.x + a[1][1][r] * inv.y);
                lds_st<unsigned short>(ta[r], (unsigned short)pk);
                lds_st<unsigned short>((r == 0 ? ta[4] : ta[r]) + 256, (unsigned short)(pk >> 16));
            }
        }
        sync4(L_MISC + 24, 8);
        {
            u32x4 d[2]; unsigned d4[2], dm[2];
#pragma unroll
            for (int c = 0; c < 2; ++c) {
                d[c] = *(LAS const u32x4*)((LAS const char*)tr0 + c * T_CH);
                d4[c] = *(LAS const unsigned short*)((LAS const char*)tr0 + c * T_CH + 16);
                dm[c] = *(LAS const unsigned short*)((LAS const char*)tr1 + c * T_CH);
            }
            lds_st<u32x4>(kz, u32x4{0u, 0u, 0u, 0u});
#pragma unroll
            for (int c = 0; c < 2; ++c) {
                u32x4 o, e;
                o.x = __builtin_amdgcn_perm(d4[c], d[c].w, s0);
                o.y = __builtin_amdgcn_perm(d4[c], tl >= 128 ? d[c].x : d[c].z, s1);
                o.z = __builtin_amdgcn_perm(d[c].z, tl < 64 ? d[c].y : d[c].x, s2);
                o.w = __builtin_amdgcn_perm(d[c].x, d[c].x, s3);
                e.x = __builtin_amdgcn_alignbit(o.y, o.x, 16); e.y = __builtin_amdgcn_alignbit(o.z, o.y, 16);
                e.z = __builtin_amdgcn_alignbit(o.w, o.z, 16); e.w = __builtin_amdgcn_alignbit(tl == 0 ? 0u : dm[c], o.w, 16);
                *(LAS u32x4*)((LAS char*)kw + 2 * c * KI_IMG) = e;
                *(LAS u32x4*)((LAS char*)kw + (2 * c + 1) * KI_IMG) = o;
            }
        }
        lds_arrive(L_MISC + 24);
        att_init();
        lds_wait(L_MISC + 24, 12);
        lds_wait(L_MISC + 32, 3);
        __builtin_amdgcn_s_setprio(0);
        {
            f32x4 y[4];
            conv_wave(abase, bbase, y);
            __builtin_amdgcn_s_setprio(2);
            lds_arrive(L_MISC + 24);
#pragma unroll
            for (int q = 0; q < 4; ++q) yq[q] = u32x2{cvtpk_bf16(y[q][0], y[q][1]), cvtpk_bf16(y[q][2], y[q][3])};
        }
    }

    unsigned rb = kh ? 20 : 4, m0b = 65536 * kh + 1024 * qp, rem = 16;
    unsigned qo0 = WS_QF + (head * 64 + 8 * qb + 2 * qp + kh) * 1024 + l16, qo1 = qo0 + 1024 - 2048 * kh;
    asm volatile("" : "+v"(qo0), "+v"(qo1));
    if (kh) lds_wait(L_MISC + 24, 16);
    if (wv == 0) {
        const int pn = g4 == 0 ? 8 + (head >> 2) : g4 == 1 ? head >> 1 : head >> 2, pm = g4 < 2 ? l15 : 2 * qb + (lane & 1);
        lds_wait(L_MISC + 16, 4);
        wait_flags<2>(wsr, WS_F2 + ((pn * 16 + pm) * 8 + xcd) * FS, tag);
        lds_arrive(L_MISC + 20);
    } else lds_wait(L_MISC + 20, 1);
    {
        q0 = ld16_raw_sc1(ws, qo0); q1 = ld16_raw_sc1(ws, qo1);
#pragma unroll
        for (int j = 0; j < 7; ++j) { dma16_sc1(m0b, P0.x, ws); m0b += 4096; P0.x += 4096; }
        if (kh) {
#pragma unroll
            for (int q = 0; q < 4; ++q) st8(wsr, yq[q], 32 * l15 + 8 * g4, WS_UT0 + 128 + (2 * b + (qp >> 1)) * UT_STRIDE + 2048 * (qp & 1) + 512 * q);
            VMWAIT(8);
            __builtin_amdgcn_s_setprio(1);
        } else VMWAIT(4);
        asm volatile("" : "+v"(q0), "+v"(q1)::"memory");
        sync4(P2.w, rb);
    }
    u32x16 W0, W1, W2, W3, XR;
    u32x8 GX, YV;
    u32x2 dsk;
    {
        const u32x8 QQ = {q0.x, q0.y, q0.z, q0.w, q1.x, q1.y, q1.z, q1.w};
        unsigned sp, sn;
        asm volatile(ATT_BODY
                     : "+{v[192:223]}"(OO), "+{v[236:251]}"(VV), "+{v[48:51]}"(P0), "+{v[52:55]}"(P1), "+{v[56:59]}"(P2), "=&{v[64:79]}"(W0), "=&{v[80:95]}"(W1),
                       "=&{v[96:111]}"(W2), "=&{v[112:127]}"(W3), "=&{v[128:143]}"(XR), "=&{v[144:151]}"(GX), "=&{v[152:159]}"(YV), "=&{v[62:63]}"(dsk),
                       [rb] "+s"(rb), [m0b] "+s"(m0b), [rem] "+s"(rem), [sp] "=&s"(sp), [sn] "=&s"(sn)
                     : "{v[224:231]}"(QQ), [msk] "s"(0x0000FFFF0000FFFFull), [ws] "s"(ws), [xb] "s"((const char*)x + T5 * 32768), [gw] "s"(gw), [pd] "s"(pD),
                       [tag] "s"(tag), [kh] "s"(kh), [pub] "s"(wv == 4 ? WS_F3 + b * FS : 0u), [lc] "i"(L_MISC + 24)
                     : "memory", "scc", "vcc", "s94", "s95", "v44", "v45", "v46", "v47", "v60", "v61", "v160", "v161", "v162", "v163", "v164", "v165", "v166", "v167", "v168", "v169", "v170", "v171", "v172", "v173", "v174", "v175", "v176", "v177", "v178", "v179", "v180", "v181", "v182", "v183", "v184", "v185", "v186", "v187", "v188", "v189", "v190", "v191", "v232", "v233", "v234", "v235");
    }
    const f32x16 O0 = __builtin_shufflevector(OO, OO, 0, 1, 2, 3, 4, 5, 6, 7, 8, 9, 10, 11, 12, 13, 14, 15);
    const f32x16 O1 = __builtin_shufflevector(OO, OO, 16, 17, 18, 19, 20, 21, 22, 23, 24, 25, 26, 27, 28, 29, 30, 31);
    const int skip = (head >> 2) == wv ? head & 3 : 4;
    const unsigned fo = WS_F4 + (T5 * 32 + 4 * wv + ((lane & 3) == skip ? (lane & 3) ^ 1 : lane & 3)) * FS;
    const char* os = ws + WS_QF + (4 * wv * 64 + T5) * 1024;
    u32x2 f4;
    asm volatile("global_load_dwordx2 %0, %1, %2 sc1" : "=v"(f4) : "v"(fo), "s"(ws) : "memory");
    {
        LAS char* exw = (LAS char*)(L_EX + wv * 2304 + lane * 16);
        LAS const char* exr = (LAS const char*)(L_EX + (wv ^ 4) * 2304 + lane * 16);
        *(LAS f32x4*)exw = f32x4{O1[0], O1[1], O1[2], O1[3]};
        *(LAS f32x4*)(exw + 1024) = f32x4{O1[4], O1[5], O1[6], O1[7]};
        *(LAS float*)(exw + 2048 - 12 * lane) = O1[8];
        BARRIER();
        const f32x4 e0 = *(LAS const f32x4*)exr, e1 = *(LAS const f32x4*)(exr + 1024);
        f32x16 O = O0;
        O[8] += *(LAS const float*)(exr + 2048 - 12 * lane);
#pragma unroll
        for (int r = 0; r < 4; ++r) { O[r] += e0[r]; O[4 + r] += e1[r]; }
        float mag = 0.f;
#pragma unroll
        for (int r = 0; r < 8; ++r) mag += __builtin_fabsf(O[r]);
        const bool bad = !(O[8] >= 0x1p-80f && O[8] <= 0x1p+80f && mag <= 3.0e38f);
        if (__builtin_expect(__builtin_amdgcn_ballot_w64(bad) != 0, 0)) {
            f32x16 z = {};
            O = z;
            float m = -3.0e38f;
            const u32x4 qf = q0, ones = {0x3f803f80u, 0x3f803f80u, 0x3f803f80u, 0x3f803f80u};
            for (int t = 0; t < 64; ++t) {
                const unsigned so = WS_KV + (head * 64 + t) * 2048;
                const u32x4 kf = ld16(wsr, lane * 16, so);
                u32x4 va = ld16(wsr, 1024 + 256 * hh + 16 * l15, so), vb = ld16(wsr, 1536 + 256 * hh + 16 * l15, so);
                if (lane & 16) { va = ones; vb = ones; }
                f32x16 S2 = __builtin_amdgcn_mfma_f32_32x32x16_bf16(as_bf8(kf), as_bf8(qf), z, 0, 0, 0);
                float tm = S2[0];
#pragma unroll
                for (int r = 1; r < 16; ++r) tm = fmaxf(tm, S2[r]);
                tm = fmaxf(tm, __shfl_xor(tm, 32));
                const float mn = fmaxf(m, tm), al = __builtin_amdgcn_exp2f(m - mn);
                m = mn;
                O *= al;
#pragma unroll
                for (int r = 0; r < 16; ++r) S2[r] = __builtin_amdgcn_exp2f(S2[r] - mn);
                const f32x8 p0 = __builtin_shufflevector(S2, S2, 0, 1, 2, 3, 4, 5, 6, 7), p1 = __builtin_shufflevector(S2, S2, 8, 9, 10, 11, 12, 13, 14, 15);
                O = __builtin_amdgcn_smfmac_f32_32x32x32_bf16(as_bf8(va), __builtin_bit_cast(bf16x16, p0), O, (int)0xDDDDDDDDu, 0, 0);
                O = __builtin_amdgcn_smfmac_f32_32x32x32_bf16(as_bf8(vb), __builtin_bit_cast(bf16x16, p1), O, (int)0xDDDDDDDDu, 0, 0);
            }
        }
        const float inv = __builtin_amdgcn_rcpf(O[8]);
        u32x4 v;
#pragma unroll
        for (int j = 0; j < 4; ++j) v[j] = cvtpk_bf16(O[2 * j] * inv, O[2 * j + 1] * inv);
        const int qt = 8 * qb + 2 * qp + kh;
        st16(wsr, v, lane * 16, WS_QF + (head * 64 + qt) * 1024);
        if (qt == T5) lds_st<u32x4>(head * 1024 + l16, v);
        asm volatile("s_waitcnt vmcnt(0)" : "+v"(f4)::"memory");
        publish(ws, WS_F4 + (qt * 32 + head) * FS, tag, lane);
    }
    {
        auto fetch_o = [&]() {
#pragma unroll
            for (int j = 0; j < 4; ++j) dma16_sc1(j == skip ? 32768 : (4 * wv + j) * 1024, l16 + (j == skip ? j ^ 1 : j) * 65536, os);
        };
        const bool early = __builtin_amdgcn_ballot_w64((((u64)f4.y << 32) | f4.x) != tag) == 0;
        if (early) fetch_o();
        __builtin_amdgcn_sched_barrier(0);
        float s[4];
#pragma unroll
        for (int i = 0; i < 4; ++i) {
            float a = 0.f;
#pragma unroll
            for (int j = 0; j < 4; ++j) a = __builtin_amdgcn_fdot2_f32_bf16(as_bf2(XR[4 * i + j]), as_bf2(GX[j]), a, false);
            s[i] = a;
        }
        const bool u5 = lane & 32, u4 = lane & 16;
#pragma unroll
        for (int i = 0; i < 2; ++i) s[i] = (u5 ? s[i + 2] : s[i]) + __shfl_xor(u5 ? s[i] : s[i + 2], 32);
        s[0] = (u4 ? s[1] : s[0]) + __shfl_xor(u4 ? s[0] : s[1], 16);
        s[0] += dpp_rot<0x128>(s[0]); s[0] += dpp_rot<0x124>(s[0]); s[0] += dpp_rot<0x122>(s[0]); s[0] += dpp_rot<0x121>(s[0]);
        const float gz = rnd_bf16(s[0]);
        const float gv = rnd_bf16(__builtin_amdgcn_rcpf(1.f + __builtin_amdgcn_exp2f(-1.44269504088896341f * gz)));
        if (l15 == 0) lds_st<float>(L_MISC + 64 + 4 * (4 * wv + g4), gv);
        float at[2][4];
#pragma unroll
        for (int tt = 0; tt < 2; ++tt)
#pragma unroll
            for (int r = 0; r < 4; ++r) {
                const float u = (r & 1) ? __uint_as_float(GX[4 + 2 * tt + (r >> 1)] & 0xffff0000u) : bf2f(GX[4 + 2 * tt + (r >> 1)] & 0xffffu);
                const float d = (r & 1) ? __uint_as_float(dsk[r >> 1] & 0xffff0000u) : bf2f(dsk[r >> 1] & 0xffffu);
                at[tt][r] = rnd_bf16(bf2f(YV[4 * tt + r]) + rnd_bf16(fmul(u, d)));
                asm volatile("" : "+v"(at[tt][r]));
            }
        unsigned oo = ((32 * T5 + l15) * 512 + n0 + 4 * g4) * 2, ob = (g4 >> 1) * 1024 + (g4 & 1) * 512 + l15 * 16;
        asm volatile("" : "+v"(oo), "+v"(ob));
        __builtin_amdgcn_sched_barrier(0);
        if (!early) { wait_flags<2>(wsr, fo, tag); fetch_o(); }
        VMWAIT(0);
        BARRIER();
        f32x4 acc[2];
        float gg[2];
        asm volatile(PROJ_BODY
                     : [a0] "=&v"(acc[0]), [a1] "=&v"(acc[1]), [g0] "=&v"(gg[0]), [g1] "=&v"(gg[1])
                     : [ob] "v"(ob), [ga] "v"(L_MISC + 64 + 4 * l15), "{v[64:79]}"(W0), "{v[80:95]}"(W1), "{v[96:111]}"(W2), "{v[112:127]}"(W3)
                     : "memory", "v160", "v161", "v162", "v163", "v164", "v165", "v166", "v167", "v168", "v169", "v170", "v171", "v172", "v173", "v174", "v175", "v176", "v177", "v178", "v179", "v180", "v181", "v182", "v183", "v184", "v185", "v186", "v187", "v188", "v189", "v190", "v191", "v192", "v193", "v194", "v195", "v196", "v197", "v198", "v199", "v200", "v201", "v202", "v203", "v204", "v205", "v206", "v207", "v208", "v209", "v210", "v211", "v212", "v213", "v214", "v215", "v216", "v217", "v218", "v219", "v220", "v221", "v222", "v223");
#pragma unroll
        for (int tt = 0; tt < 2; ++tt) {
            float o[4];
#pragma unroll
            for (int r = 0; r < 4; ++r) o[r] = __builtin_fmaf(gg[tt], at[tt][r] - acc[tt][r], acc[tt][r]);
            *(GAS u32x2*)((GAS char*)out + oo + tt * 16384) = u32x2{cvtpk_bf16(o[0], o[1]), cvtpk_bf16(o[2], o[3])};
        }
        asm volatile(".Lgm_end:" ::: "memory");
    }
}

extern "C" void kernel_launch(void* const* d_in, const int* in_sizes, int n_in, void* d_out, int out_size, void* d_ws, size_t ws_size,
                              hipStream_t stream) {
    (void)in_sizes; (void)n_in; (void)out_size; (void)ws_size;
    gated_mix_kernel<<<256, 512, 0, stream>>>((cptr16)d_in[0], (cptr16)d_in[10], (char*)d_ws, (cptr16)d_in[1], (cptr16)d_in[2], (cptr16)d_in[7],
                                               (cptr16)d_in[3], (cptr16)d_in[4], (cptr16)d_in[5], (cptr16)d_in[6], (cptr16)d_in[11],
                                               (cptr16)d_in[8], (cptr16)d_in[12], (unsigned short*)d_out);
}
```
